```python
import torch

def _softmax(x, axis=-1):
    return torch.softmax(x, dim=axis)
def _gelu(x):
    return torch.nn.functional.gelu(x, approximate="tanh")
def _silu(x):
    return torch.nn.functional.silu(x)
def _sigmoid(x):
    return torch.sigmoid(x)
def _layernorm(x, axis=-1, eps=1e-5):
    m = x.mean(dim=axis, keepdim=True)
    v = x.var(dim=axis, keepdim=True, unbiased=False)
    return (x - m) / torch.sqrt(v + eps)
def _scatter_add(tgt, idx, src, axis=0):
    return tgt.clone().index_add_(axis, idx, src)
def _segment_sum(src, seg, n):
    out = torch.zeros((n,) + src.shape[1:], dtype=src.dtype)
    return out.index_add_(0, seg, src)
def _pad(x, pw):
    flat = tuple(v for pair in reversed(pw) for v in pair)
    return torch.nn.functional.pad(x, flat)

kernel_name = 'interleave_layers__spectral_fnet__swiglu_ffn__prompt'

def setup_inputs():

    _w = (lambda a: (lambda t: t.to(torch.bfloat16) if t.is_floating_point() else t)(torch.as_tensor(a)))
    def _A_setup():
        B, S, D = 1, 2048, 448
        ns = torch.arange(S)[:, None] * torch.arange(S)[None, :]
        nd = torch.arange(D)[:, None] * torch.arange(D)[None, :]
        Ws_re = torch.cos(-2.0 * torch.pi * ns / S).to(float)
        Ws_im = torch.sin(-2.0 * torch.pi * ns / S).to(float)
        Wd_re = torch.cos(-2.0 * torch.pi * nd / D).to(float)
        Wd_im = torch.sin(-2.0 * torch.pi * nd / D).to(float)
        return {
            "x": _w((lambda *s: torch.randn(*s, dtype=torch.bfloat16) * 0.05)(B, S, D)),
            "dft_seq_re": _w(Ws_re),
            "dft_seq_im": _w(Ws_im),
            "dft_hid_re": _w(Wd_re),
            "dft_hid_im": _w(Wd_im),
        }
    _A_in = _A_setup()

    def _B_setup():
        B, S, D, F = 1, 2048, 448, 1792
        return {
            "x": _w((lambda *s: torch.randn(*s, dtype=torch.bfloat16) * 0.05)(B, S, D)),
            "w_gate": _w((lambda *s: torch.randn(*s, dtype=torch.bfloat16) * 0.4)(D, F)),
            "w_up": _w((lambda *s: torch.randn(*s, dtype=torch.bfloat16) * 0.4)(D, F)),
            "w_down": _w((lambda *s: torch.randn(*s, dtype=torch.bfloat16) * 0.4)(F, D)),
        }
    _B_in = _B_setup()

    return {
        "A_x": _A_in["x"],
        "A_dft_seq_re": _A_in["dft_seq_re"],
        "A_dft_seq_im": _A_in["dft_seq_im"],
        "A_dft_hid_re": _A_in["dft_hid_re"],
        "A_dft_hid_im": _A_in["dft_hid_im"],
        "B_x": _B_in["x"],
        "B_w_gate": _B_in["w_gate"],
        "B_w_up": _B_in["w_up"],
        "B_w_down": _B_in["w_down"],
    }

def reference(A_x, A_dft_seq_re, A_dft_seq_im, A_dft_hid_re, A_dft_hid_im, B_x, B_w_gate, B_w_up, B_w_down):
    def _A_ref(x, dft_seq_re, dft_seq_im, dft_hid_re, dft_hid_im):
        a = torch.einsum("bsd,ed->bse", x, dft_hid_re)
        b = torch.einsum("bsd,ed->bse", x, dft_hid_im)
        y = torch.einsum("ts,bse->bte", dft_seq_re, a) - torch.einsum("ts,bse->bte", dft_seq_im, b)
        return y

    def _B_ref(x, w_gate, w_up, w_down):
        g = _silu(x @ w_gate)
        u = x @ w_up
        return (g * u) @ w_down

    x = A_x
    x = x + _A_ref(_layernorm(x, axis=-1), A_dft_seq_re, A_dft_seq_im, A_dft_hid_re, A_dft_hid_im)
    x = x + _B_ref(_layernorm(x, axis=-1), B_w_gate, B_w_up, B_w_down)
    x = x + _A_ref(_layernorm(x, axis=-1), A_dft_seq_re, A_dft_seq_im, A_dft_hid_re, A_dft_hid_im)
    x = x + _B_ref(_layernorm(x, axis=-1), B_w_gate, B_w_up, B_w_down)
    return x
```

```cpp
#include <hip/hip_runtime.h>
#include <hip/hip_bf16.h>
#include <stdint.h>
#include <type_traits>

namespace fk {

#define LAS __attribute__((address_space(3)))
#define GAS __attribute__((address_space(1)))
typedef unsigned u32;
typedef unsigned long long u64;
typedef u32 u32x4 __attribute__((ext_vector_type(4)));
typedef u32 u32x2 __attribute__((ext_vector_type(2)));
typedef int i32x4 __attribute__((ext_vector_type(4)));
typedef short bf16x8 __attribute__((ext_vector_type(8)));
typedef float f32x4 __attribute__((ext_vector_type(4)));
typedef float f32x2 __attribute__((ext_vector_type(2)));

constexpr int SEQ = 2048, DM = 448, FF = 1792;
constexpr int NWG = 256, NTHR = 768;
constexpr float LN_EPS = 1e-5f;

constexpr u32 OFF_SALT = 0, OFF_ABORT = 256;
constexpr u32 OFF_FP0A = 4096, OFF_FP0B = 8192, OFF_FR = 12288;
constexpr u32 OFF_FS = 16384, OFF_FH = 24576, OFF_FG = 32768;
constexpr u32 OFF_CS = 65536;
constexpr u32 OFF_STATS = OFF_CS + 16384;
constexpr u32 STATS_L = SEQ * 4 * 8;
constexpr u32 OFF_WH = OFF_STATS + 2 * STATS_L;
constexpr u32 WH_CC = 196 * 1024;
constexpr u32 OFF_WFF = OFF_WH + 4 * WH_CC;
constexpr u32 WFF_FS = 588 * 1024;
constexpr u32 ACT_B = SEQ * DM * 2;
constexpr u32 OFF_XN = OFF_WFF + 8 * WFF_FS;
constexpr u32 OFF_PQ = OFF_XN + 2 * ACT_B;
constexpr u32 PQ_L = 4 * ACT_B;
constexpr u32 OFF_X1R = OFF_PQ + 2 * PQ_L;
constexpr u32 OFF_PART = OFF_X1R + 2 * ACT_B;
constexpr u32 PART_FS = 128 * 16 * 1024, PART_L = 8 * PART_FS;
constexpr u32 OFF_X2 = OFF_PART + 2 * PART_L;
constexpr u32 WS_USED = OFF_X2 + ACT_B;

#define FK_SA_N 5
#define FK_SB_N 5
constexpr u32 L_SA = 0, SA_ST = 16384;            constexpr int SA_N = FK_SA_N;
constexpr u32 L_SB = SA_N * SA_ST, SB_ST = 14336; constexpr int SB_N = FK_SB_N;
constexpr u32 L_HW = 0;                           constexpr int HW_CAP = 39;
constexpr u32 L_RED = 102400;
constexpr u32 L_GW = 0;                           constexpr int GW_CAP = 25;
constexpr u32 L_GX = 102400;
constexpr u32 L_SCR = 114688;
constexpr u32 L_T = L_SCR + 30720;  constexpr u32 T_PITCH = 912;
constexpr u32 L_MISC = 159744;
constexpr u32 LDS_BYTES = 163840;
static_assert(L_SB + SB_N * SB_ST <= L_MISC && SA_N * SA_ST <= L_SCR && 4 * HW_CAP * 1024 <= L_MISC && 4 * GW_CAP * 1024 <= L_GX, "LDS map");

struct Params {
  const void* x0; const void* Cr; const void* Ci; const void* Wr; const void* Wi;
  const void* Wg; const void* Wu; const void* Wd; void* out; char* ws;
};

template <int LO, int HI, class F> __device__ __forceinline__ void sfor(F&& f) {
  if constexpr (HI - LO == 1) f(std::integral_constant<int, LO>{});
  else if constexpr (HI - LO > 1) { sfor<LO, (LO + HI) / 2>(f); sfor<(LO + HI) / 2, HI>(f); }
}
#define BAR() asm volatile("s_barrier" ::: "memory")
#define TR_ARG
#define TR_ARG0
#define TR_PASS
#define TR_PASS0
#define WAIT_L0() asm volatile("s_waitcnt lgkmcnt(0)" ::: "memory")
#define WAIT_V0() asm volatile("s_waitcnt vmcnt(0)" ::: "memory")
template <int N> __device__ __forceinline__ void wait_v() { asm volatile("s_waitcnt vmcnt(%0)" ::"n"(N > 63 ? 63 : N) : "memory"); }
#define SCHED_FENCE() __builtin_amdgcn_sched_barrier(0)
constexpr int AUX_SC1 = 16;

__device__ __forceinline__ i32x4 make_rsrc(const void* p, u32 bytes) {
  const u64 a = (u64)p;
  i32x4 r;
  r.x = __builtin_amdgcn_readfirstlane((int)(u32)a);
  r.y = __builtin_amdgcn_readfirstlane((int)(u32)(a >> 32) & 0xffff);
  r.z = (int)bytes;
  r.w = 0x00020000;
  return r;
}
template <bool SC1, u32 LIMM, u32 SIMM> __device__ __forceinline__ void dma16(u32 lbase, u32 voff, i32x4 rs) {
  u32 tmp;
  if constexpr (SC1) asm volatile("s_add_u32 m0, %1, %4\n\ts_mov_b32 %0, %5\n\tbuffer_load_dwordx4 %2, %3, %0 offen sc1 lds" : "=&s"(tmp) : "s"(lbase), "v"(voff), "s"(rs), "n"(LIMM), "n"(SIMM) : "scc");
  else asm volatile("s_add_u32 m0, %1, %4\n\ts_mov_b32 %0, %5\n\tbuffer_load_dwordx4 %2, %3, %0 offen lds" : "=&s"(tmp) : "s"(lbase), "v"(voff), "s"(rs), "n"(LIMM), "n"(SIMM) : "scc");
}
__device__ __forceinline__ u32 pk_bf16(float lo, float hi) {
  typedef __bf16 bf2 __attribute__((ext_vector_type(2)));
  bf2 r; r.x = (__bf16)lo; r.y = (__bf16)hi;
  return __builtin_bit_cast(u32, r);
}
__device__ __forceinline__ float as_f(u32 v) { return __builtin_bit_cast(float, v); }
__device__ __forceinline__ float bf_lo(u32 v) { return __builtin_bit_cast(float, v << 16); }
__device__ __forceinline__ float bf_hi(u32 v) { return __builtin_bit_cast(float, v & 0xffff0000u); }
__device__ __forceinline__ f32x4 mfma(bf16x8 a, bf16x8 b, f32x4 c) { return __builtin_amdgcn_mfma_f32_16x16x32_bf16(a, b, c, 0, 0, 0); }
template <int CTRL> __device__ __forceinline__ float dpp_add(float x) {
  return x + __builtin_bit_cast(float, __builtin_amdgcn_update_dpp(0, __builtin_bit_cast(int, x), CTRL, 0xf, 0xf, true));
}
__device__ __forceinline__ float sum16(float x) {
  x = dpp_add<0xB1>(x);
  x = dpp_add<0x4E>(x);
  x = dpp_add<0x141>(x);
  x = dpp_add<0x140>(x);
  return x;
}
__device__ __forceinline__ float sum64(float x) {
  x = sum16(x);
  const int xi = __builtin_bit_cast(int, x);
  const float a = __builtin_bit_cast(float, __builtin_amdgcn_readlane(xi, 0)), b = __builtin_bit_cast(float, __builtin_amdgcn_readlane(xi, 16));
  const float c = __builtin_bit_cast(float, __builtin_amdgcn_readlane(xi, 32)), d = __builtin_bit_cast(float, __builtin_amdgcn_readlane(xi, 48));
  return (a + b) + (c + d);
}
__device__ __forceinline__ int fresh(int v) { asm volatile("" : "+v"(v)); return v; }
__device__ __forceinline__ u32 freshu(u32 v) { asm volatile("" : "+v"(v)); return v; }
typedef __amdgpu_buffer_rsrc_t rsrc_t;
__device__ __forceinline__ u32x4 ld16_sc1(rsrc_t r, u32 off) { return __builtin_amdgcn_raw_buffer_load_b128(r, (int)off, 0, AUX_SC1); }
__device__ __forceinline__ void st16_sc1(rsrc_t r, u32 off, u32x4 v) { __builtin_amdgcn_raw_buffer_store_b128(v, r, (int)off, 0, AUX_SC1); }
__device__ __forceinline__ void st8_sc1(rsrc_t r, u32 off, u32x2 v) { __builtin_amdgcn_raw_buffer_store_b64(v, r, (int)off, 0, AUX_SC1); }
template <class T> __device__ __forceinline__ T lds_ld(LAS char* l, u32 off) { return *(const LAS T*)(l + off); }
template <class T> __device__ __forceinline__ void lds_st(LAS char* l, u32 off, T v) { *(LAS T*)(l + off) = v; }

struct Roles {
  int b, s_kh, s_tt, s_ec, h_rt, h_cc, g_rb, g_fs;
  __device__ __forceinline__ explicit Roles(int b_) {
    b = b_;
    const int x = b & 7, j = b >> 3;
    s_kh = x & 1; s_tt = (x >> 1) * 8 + (j >> 2); s_ec = j & 3;
    h_rt = x * 8 + (j >> 2); h_cc = j & 3;
    g_fs = (x & 3) * 2 + (j & 1); g_rb = (x >> 2) * 16 + (j >> 1);
  }
};

enum : int { OP_BAR, OP_WAIT, OP_SA, OP_SB, OP_HW, OP_GX, OP_GW, OP_SAP };
struct Op { int kind, a, b; };
struct Prog { Op op[1500]; int n; };
struct Builder {
  Prog p{}; int issued = 0;
  constexpr void push(int k, int a = 0, int b = 0) { p.op[p.n].kind = k; p.op[p.n].a = a; p.op[p.n].b = b; ++p.n; }
  constexpr int dma(int k, int a, int b = 0) { push(k, a, b); return issued++; }
  constexpr void bar() { push(OP_BAR); }
  constexpr void wait_for(int pos) { push(OP_WAIT, imax_(issued - 1 - pos, 0)); }
  static constexpr int imax_(int a, int b) { return a > b ? a : b; }
};
constexpr int imax(int a, int b) { return a > b ? a : b; }
constexpr int imin(int a, int b) { return a < b ? a : b; }
constexpr int S_BARS = 19, H_BARS = 11, G_BARS = 26, R_BARS = 4, P0_BARS = 4;
constexpr int GD_NST = 22;
constexpr int gd_end(int sg) { return sg < 14 ? 7 * (sg + 1) : sg < 17 ? 98 + 8 * (sg - 13) : sg == 17 ? 126 : sg < 21 ? 126 + 6 * (sg - 17) : 147; }
constexpr int gd_begin(int sg) { return sg == 0 ? 0 : gd_end(sg - 1); }
constexpr int gd_stage_of(int n) { int sg = 0; while (gd_end(sg) <= n) ++sg; return sg; }

#define FK_HW_FROM 11
#define FK_GW_FROM 2
#define FK_SAP_AT 1

constexpr Prog build_layer() {
  Builder B{};
  int posSA[16] = {}, posSB[16] = {};
  bool busy[160] = {};
  for (int s = 0; s < SA_N; ++s) for (int i = 0; i < 8; ++i) posSA[s] = B.issued++;
  int posHW[49] = {}; bool doneHW[49][2] = {}; int nHW = 0;
  int posGW[147] = {}; bool doneGW[147][2] = {}; int nGW = 0;
  auto hw_pre = [&]() { for (int n = 0; n < HW_CAP; ++n) for (int ws = 0; ws < 2; ++ws)
      if (!doneHW[n][ws] && !busy[ws * HW_CAP + n] && !busy[(2 + ws) * HW_CAP + n]) { posHW[n] = imax(posHW[n], B.dma(OP_HW, n, ws)); doneHW[n][ws] = true; } };
  auto hw_upto = [&](int lim) { for (; nHW < imin(49, lim); ++nHW) for (int ws = 0; ws < 2; ++ws)
      if (!doneHW[nHW][ws]) { posHW[nHW] = imax(posHW[nHW], B.dma(OP_HW, nHW, ws)); doneHW[nHW][ws] = true; } };
  auto gw_pre = [&]() { for (int n = 0; n < GW_CAP; ++n) for (int ws = 0; ws < 2; ++ws)
      if (!doneGW[n][ws] && !busy[ws * GW_CAP + n] && !busy[(2 + ws) * GW_CAP + n]) { posGW[n] = imax(posGW[n], B.dma(OP_GW, n, ws)); doneGW[n][ws] = true; } };
  auto gw_upto = [&](int lim) { for (; nGW < imin(147, lim); ++nGW) for (int ws = 0; ws < 2; ++ws)
      if (!doneGW[nGW][ws]) { posGW[nGW] = imax(posGW[nGW], B.dma(OP_GW, nGW, ws)); doneGW[nGW][ws] = true; } };
  auto need = [&](const int* pos, int lo, int hi) { int m = 0; for (int n = lo; n < hi; ++n) m = imax(m, pos[n]); return m; };
  for (int k = 0; k < 160; ++k) busy[k] = true;
  B.bar();
  for (int s = 0; s < SB_N; ++s) for (int i = 0; i < 7; ++i) posSB[s] = B.dma(OP_SB, s, i);
  B.wait_for(imax(posSA[0], posSB[0])); B.bar();
  for (int s = 0; s < 16; ++s) {
    if (s < 15) B.wait_for(imax(posSA[s + 1], posSB[s + 1]));
    B.bar();
    if (s + SB_N < 16) { for (int i = 0; i < 7; ++i) posSB[s + SB_N] = B.dma(OP_SB, s + SB_N, i); }
    else for (int k = 0; k < 14; ++k) busy[L_SB / 1024 + (s % SB_N) * 14 + k] = false;
    if (s + SA_N < 16) { for (int i = 0; i < 8; ++i) posSA[s + SA_N] = B.dma(OP_SA, s + SA_N, i); }
    else for (int k = 0; k < 16; ++k) busy[(s % SA_N) * 16 + k] = false;
    if (s == 15) for (int k = 0; k < 160; ++k) busy[k] = false;
    if (s >= FK_HW_FROM) hw_pre();
  }
  B.bar();
  hw_upto(HW_CAP);
  for (int k = 0; k < 160; ++k) busy[k] = true;
  B.bar();
  B.wait_for(need(posHW, 0, 7)); B.bar();
  for (int t = 0; t < 7; ++t) {
    if (t < 6) B.wait_for(need(posHW, 7 * (t + 1), 7 * (t + 2)));
    B.bar();
    hw_upto(7 * (t + 1) + HW_CAP);
    for (int n = 7 * t; n < 7 * t + 7; ++n) if (n + HW_CAP >= 49) for (int c = 0; c < 4; ++c) busy[c * HW_CAP + n % HW_CAP] = false;
    if (t >= FK_GW_FROM) gw_pre();
  }
  B.bar();
  B.bar();
  gw_upto(GW_CAP);
  for (int k = 0; k < 160; ++k) busy[k] = true;
  B.bar();
  int posGX = 0;
  for (int i = 0; i < 28; ++i) posGX = B.dma(OP_GX, i);
  B.wait_for(imax(posGX, need(posGW, 0, 7))); B.bar();
  bool doneSAP[SA_N * 8] = {};
  auto sap = [&]() { for (int n = 0; n < SA_N * 8; ++n)
      if (!doneSAP[n] && !busy[(n / 8) * 16 + n % 8] && !busy[(n / 8) * 16 + 8 + n % 8]) { B.push(OP_SAP, n / 8, n % 8); doneSAP[n] = true; } };
  for (int sg = 0; sg < GD_NST; ++sg) {
    if (sg + 1 < GD_NST && sg != 13) B.wait_for(need(posGW, gd_begin(sg + 1), gd_end(sg + 1)));
    B.bar();
    gw_upto(gd_end(sg) + GW_CAP);
    for (int n = gd_begin(sg); n < gd_end(sg); ++n) if (n + GW_CAP >= 147) for (int c = 0; c < 4; ++c) busy[c * GW_CAP + n % GW_CAP] = false;
    if (sg == 13) { B.wait_for(need(posGW, gd_begin(14), gd_end(14))); B.bar(); }
    if (!FK_SAP_AT) sap();
  }
  B.bar();
  for (int k = 0; k < 160; ++k) busy[k] = false;
  sap();
  for (int i = 0; i < R_BARS; ++i) B.bar();
  return B.p;
}
constexpr Prog LAYER_PROG = build_layer();
constexpr int count_bars(const Prog& p) { int c = 0; for (int i = 0; i < p.n; ++i) c += p.op[i].kind == OP_BAR; return c; }
static_assert(count_bars(LAYER_PROG) == S_BARS + H_BARS + G_BARS + R_BARS, "loader and compute barrier counts differ");

__device__ __forceinline__ void loader_role(const Params& p, const Roles& R, const int l, const int lane TR_ARG) {
  const i32x4 rC = make_rsrc(l ? p.Ci : p.Cr, SEQ * SEQ * 2);
  const i32x4 rW = make_rsrc(p.ws, WS_USED);
  u32 vSA[2];
#pragma unroll
  for (int pr = 0; pr < 2; ++pr)
    vSA[pr] = (u32)((R.s_tt * 64 + (lane >> 3)) * (SEQ * 2) + R.s_kh * 2048 + (((lane & 7) ^ ((4 * pr + (lane >> 4)) & 7)) * 16));
  const u32 l16 = (u32)lane * 16;
  const u32 vHW = l16 + l * 14336 + OFF_WH + R.h_cc * WH_CC;
  const u32 vGW = l16 + l * 2048 + OFF_WFF + R.g_fs * WFF_FS;
  const u32 bSA = L_SA + l * 8192, bSB = L_SB + l * 7168, bHW = L_HW + l * 2 * HW_CAP * 1024, bGW = L_GW + l * 2 * GW_CAP * 1024;
  const u32 bGX = L_GX + l * 4096;
  auto sa = [&](auto sc, auto ic) { constexpr int s = sc, i = ic; dma16<false, (s % SA_N) * SA_ST + i * 1024, s * 128 + i * 32768>(bSA, vSA[i & 1], rC); };
  auto sa_prefetch = [&]() { sfor<0, SA_N * 8>([&](auto n) { sa(std::integral_constant<int, n / 8>{}, std::integral_constant<int, n % 8>{}); }); };

  sa_prefetch();
  sfor<0, P0_BARS>([&](auto) { BAR(); });
#pragma unroll 1
  for (int layer = 0; layer < 2; ++layer) {
    const u32 vSB = l16 + l * 7168 + OFF_XN + layer * ACT_B + R.s_ec * (64 * 7168) + R.s_kh * (32 * 7168);
    u32 vGX[2];
#pragma unroll
    for (int pr = 0; pr < 2; ++pr)
      vGX[pr] = OFF_X1R + layer * ACT_B + (u32)((R.g_rb * 64 + 32 * l + (lane >> 3)) * (DM * 2) + (((lane & 7) ^ ((4 * pr + (lane >> 4)) & 7)) * 16));
    sfor<0, LAYER_PROG.n>([&](auto ic) {
      constexpr Op o = LAYER_PROG.op[ic];
      if constexpr (o.kind == OP_BAR) BAR();
      else if constexpr (o.kind == OP_WAIT) wait_v<o.a>();
      else if constexpr (o.kind == OP_SA) sa(std::integral_constant<int, o.a>{}, std::integral_constant<int, o.b>{});
      else if constexpr (o.kind == OP_SB) dma16<true, (o.a % SB_N) * SB_ST + o.b * 1024, o.a * 14336 + o.b * 1024>(bSB, vSB, rW);
      else if constexpr (o.kind == OP_HW) dma16<true, (o.b * HW_CAP + o.a % HW_CAP) * 1024, ((o.a / 7) * 28 + 7 * o.b + o.a % 7) * 1024>(bHW, vHW, rW);
      else if constexpr (o.kind == OP_GX) dma16<true, (o.a % 7) * 8192 + (o.a / 7) * 1024, (o.a / 7) * 8 * (DM * 2) + (o.a % 7) * 128>(bGX, vGX[(o.a / 7) & 1], rW);
      else if constexpr (o.kind == OP_GW) dma16<true, (o.b * GW_CAP + o.a % GW_CAP) * 1024, (o.a * 4 + o.b) * 1024>(bGW, vGW, rW);
      else if constexpr (o.kind == OP_SAP) { if (layer == 0) sa(std::integral_constant<int, o.a>{}, std::integral_constant<int, o.b>{}); }
    });
  }
}

struct Sync {
  GAS char* ws; u64 salt; bool dead; int lane;
  __device__ __forceinline__ void give_up() {
    if (lane == 0) __hip_atomic_store((GAS u64*)(ws + OFF_ABORT), salt + 63, __ATOMIC_RELAXED, __HIP_MEMORY_SCOPE_AGENT);
    dead = true;
  }
  template <bool TWO> __device__ __forceinline__ void poll(u32 off, int n, u64 tag) {
    if (dead) return;
    const u64 t0 = __builtin_amdgcn_s_memrealtime();
    for (u32 spins = 1;; ++spins) {
      u64 v = tag, v2 = tag;
      if (lane < n) v = __hip_atomic_load((GAS u64*)(ws + off + lane * 8), __ATOMIC_RELAXED, __HIP_MEMORY_SCOPE_AGENT);
      if (TWO) v2 = __hip_atomic_load((GAS u64*)(ws + off + (lane + 64) * 8), __ATOMIC_RELAXED, __HIP_MEMORY_SCOPE_AGENT);
      if (__all(v == tag && v2 == tag)) break;
      if ((spins & 63u) == 0u) {
        const u64 ab = __hip_atomic_load((GAS u64*)(ws + OFF_ABORT), __ATOMIC_RELAXED, __HIP_MEMORY_SCOPE_AGENT);
        if (__all(ab == salt + 63)) { dead = true; return; }
        if (__builtin_amdgcn_s_memrealtime() - t0 > 400000ull) { give_up(); return; }
      }
    }
    __builtin_amdgcn_fence(__ATOMIC_ACQUIRE, "wavefront");
  }
  __device__ __forceinline__ void post(u32 off, u64 tag) {
    if (lane == 0) __hip_atomic_store((GAS u64*)(ws + off), tag, __ATOMIC_RELAXED, __HIP_MEMORY_SCOPE_AGENT);
  }
};
template <int N> __device__ __forceinline__ void bars(TR_ARG0) { sfor<0, N>([&](auto) { BAR(); }); }

#define STAMP(k) do { } while (0)
__device__ __forceinline__ void sync_role(const Params& p, const Roles& R, const bool active, const int lane TR_ARG) {
  Sync sy; sy.ws = (GAS char*)p.ws; sy.lane = lane; sy.dead = !active; sy.salt = 0;
  STAMP(0);
  if (active) sy.salt = __hip_atomic_load((GAS u64*)(sy.ws + OFF_SALT), __ATOMIC_RELAXED, __HIP_MEMORY_SCOPE_AGENT);
  const u64 salt = sy.salt;
  bars<2>(TR_PASS0); if (active) sy.post(OFF_FP0A + R.b * 8, salt + 1);
  STAMP(1);
  bars<2>(TR_PASS0); if (active) sy.post(OFF_FP0B + R.b * 8, salt + 2);
  STAMP(2);
#pragma unroll 1
  for (int layer = 0; layer < 2; ++layer) {
    const int sb = 3 + layer * 16; (void)sb;
    sy.template poll<true>((layer ? OFF_FR : OFF_FP0A) + R.s_kh * 1024, 64, layer ? salt + 3 : salt + 1);
    STAMP(sb + 0);
    bars<2>(TR_PASS0);
    STAMP(sb + 1);
    if (layer == 0) {
      sy.template poll<true>(OFF_FP0B, 64, salt + 2);
      sy.template poll<true>(OFF_FP0B + 1024, 64, salt + 2);
      if (R.b == 0 && !sy.dead) sy.post(OFF_SALT, salt + 64);
    }
    bars<S_BARS - 3>(TR_PASS0);
    STAMP(sb + 2);
    bars<1>(TR_PASS0);
    if (active) sy.post(OFF_FS + (layer * 32 + R.s_tt) * 128 + (R.s_kh * 4 + R.s_ec) * 8, salt + 4 + layer);
    STAMP(sb + 3);
    sy.template poll<false>(OFF_FS + (layer * 32 + (R.h_rt >> 1)) * 128, 8, salt + 4 + layer);
    STAMP(sb + 4);
    bars<2>(TR_PASS0);
    STAMP(sb + 5);
    bars<7>(TR_PASS0);
    STAMP(sb + 6);
    bars<H_BARS - 9>(TR_PASS0);
    if (active) sy.post(OFF_FH + (layer * 32 + (R.h_rt >> 1)) * 128 + ((R.h_rt & 1) * 4 + R.h_cc) * 8, salt + 6 + layer);
    STAMP(sb + 7);
    sy.template poll<false>(OFF_FH + (layer * 32 + R.g_rb) * 128, 8, salt + 6 + layer);
    STAMP(sb + 8);
    bars<2>(TR_PASS0);
    STAMP(sb + 9);
    bars<14>(TR_PASS0);
    STAMP(sb + 10);
    bars<1>(TR_PASS0);
    STAMP(sb + 11);
    bars<8>(TR_PASS0);
    STAMP(sb + 12);
    bars<G_BARS - 25>(TR_PASS0);
    if (active) sy.post(OFF_FG + (layer * 32 + R.g_rb) * 128 + R.g_fs * 8, salt + 8 + layer);
    STAMP(sb + 13);
    sy.template poll<false>(OFF_FG + (layer * 32 + (R.b >> 3)) * 128, 8, salt + 8 + layer);
    STAMP(sb + 14);
    bars<R_BARS>(TR_PASS0);
    if (active && layer == 0) sy.post(OFF_FR + R.b * 8, salt + 3);
    STAMP(sb + 15);
  }
}

__device__ __forceinline__ void ln_row_to_lds(LAS char* L, const float (&x)[8], int w, int lane) {
  const bool on = lane < 56;
  float s = 0.f;
#pragma unroll
  for (int j = 0; j < 8; ++j) s += x[j];
  const float mean = sum64(on ? s : 0.f) * (1.f / DM);
  float q = 0.f;
#pragma unroll
  for (int j = 0; j < 8; ++j) { const float d = x[j] - mean; q += d * d; }
  const float rstd = __builtin_amdgcn_rsqf(sum64(on ? q : 0.f) * (1.f / DM) + LN_EPS);
  u32x4 o;
#pragma unroll
  for (int j = 0; j < 4; ++j) o[j] = pk_bf16((x[2 * j] - mean) * rstd, (x[2 * j + 1] - mean) * rstd);
  if (on) lds_st<u32x4>(L, L_T + w * T_PITCH + lane * 16, o);
}
__device__ __forceinline__ void xn_store(LAS char* L, rsrc_t rW, u32 base, int b, int e) {
  if (e < DM) {
    u32 h[8];
#pragma unroll
    for (int j = 0; j < 8; ++j) h[j] = lds_ld<unsigned short>(L, L_T + j * T_PITCH + e * 2);
    const int ec = e / 112, T = (e % 112) >> 4, el = e & 15;
    st16_sc1(rW, base + (u32)(((ec * 64 + (b >> 2)) * 7 + T) * 1024 + ((b & 3) * 16 + el) * 16),
             u32x4{h[0] | (h[1] << 16), h[2] | (h[3] << 16), h[4] | (h[5] << 16), h[6] | (h[7] << 16)});
  }
}

__device__ __forceinline__ void p0_comp(const Params& p, const Roles& R, LAS char* L, rsrc_t rW, int w, int lane TR_ARG) {
  const int tid = w * 64 + lane, b = R.b;
  u32x4 xr = {0, 0, 0, 0};
  if (lane < 56) xr = *(const GAS u32x4*)((const GAS char*)p.x0 + (size_t)(8 * b + w) * (DM * 2) + lane * 16);
  const int kind = b < 112 ? 0 : b < 168 ? 1 : b < 196 ? 2 : b < 252 ? 3 : 4;
  u32x4 it[7];
#pragma unroll
  for (int i = 0; i < 7; ++i) it[i] = u32x4{0, 0, 0, 0};
  if (kind == 0) {
    const int fs = b / 14, ks = b % 14;
#pragma unroll
    for (int i = 0; i < 4; ++i) { const int c = tid + 512 * i, m = c / 896, r = (c % 896) / 28, ch = c % 28;
      if (c < 1792) it[i] = *(const GAS u32x4*)((const GAS char*)(m ? p.Wu : p.Wg) + (size_t)(32 * ks + r) * (FF * 2) + fs * 448 + ch * 16); }
  } else if (kind == 1) {
    const int fs = (b - 112) / 7, k = (b - 112) % 7;
#pragma unroll
    for (int i = 0; i < 4; ++i) { const int c = tid + 512 * i, r = c / 56, ch = c % 56, q = r >> 3, j = r & 7;
      const int f = 224 * fs + 8 * (7 * (j >> 1) + k) + 2 * q + (j & 1);
      if (c < 1792) it[i] = *(const GAS u32x4*)((const GAS char*)p.Wd + (size_t)f * (DM * 2) + ch * 16); }
  } else if (kind == 2) {
    const int cc = (b - 168) / 7, tp = (b - 168) % 7;
#pragma unroll
    for (int i = 0; i < 4; ++i) { const int c = tid + 512 * i, m = c / 896, r = (c % 896) / 56, ch = c % 56;
      if (c < 1792) it[i] = *(const GAS u32x4*)((const GAS char*)(m ? p.Wi : p.Wr) + (size_t)(112 * cc + 16 * tp + r) * (DM * 2) + ch * 16); }
  } else if (kind == 3) {
    const int it0 = b - 196, rg = tid >> 3, m = (tid >> 2) & 1, ch = tid & 3;
#pragma unroll
    for (int i = 0; i < 7; ++i) it[i] = *(const GAS u32x4*)((const GAS char*)(m ? p.Wu : p.Wg) + (size_t)(7 * rg + i) * (FF * 2) + it0 * 64 + ch * 16);
  }
  float x[8];
#pragma unroll
  for (int j = 0; j < 4; ++j) { x[2 * j] = bf_lo(xr[j]); x[2 * j + 1] = bf_hi(xr[j]); }
  ln_row_to_lds(L, x, w, lane);
  WAIT_L0(); BAR();
  xn_store(L, rW, OFF_XN, b, tid);
  WAIT_V0(); BAR();
  if (kind == 0) {
#pragma unroll
    for (int i = 0; i < 4; ++i) { const int c = tid + 512 * i, m = c / 896, r = (c % 896) / 28, ch = c % 28; if (c < 1792) lds_st<u32x4>(L, L_SCR + (m * 32 + r) * 464 + ch * 16, it[i]); }
  } else if (kind == 1) {
#pragma unroll
    for (int i = 0; i < 4; ++i) { const int c = tid + 512 * i, r = c / 56, ch = c % 56; if (c < 1792) lds_st<u32x4>(L, L_SCR + r * 912 + ch * 16, it[i]); }
  } else if (kind == 2) {
#pragma unroll
    for (int i = 0; i < 4; ++i) { const int c = tid + 512 * i, m = c / 896, r = (c % 896) / 56, ch = c % 56; if (c < 1792) lds_st<u32x4>(L, L_SCR + (m * 16 + r) * 912 + ch * 16, it[i]); }
  } else if (kind == 3) {
    float s[8] = {0, 0, 0, 0, 0, 0, 0, 0};
#pragma unroll
    for (int i = 0; i < 7; ++i)
#pragma unroll
      for (int j = 0; j < 4; ++j) { s[2 * j] += bf_lo(it[i][j]); s[2 * j + 1] += bf_hi(it[i][j]); }
    lds_st<f32x4>(L, L_SCR + tid * 32, f32x4{s[0], s[1], s[2], s[3]});
    lds_st<f32x4>(L, L_SCR + tid * 32 + 16, f32x4{s[4], s[5], s[6], s[7]});
  }
  WAIT_L0(); BAR();
  if (kind == 0) {
    const int fs = b / 14, ks = b % 14;
#pragma unroll
    for (int i = 0; i < 4; ++i) { const int c = tid + 512 * i, tau = c >> 6, ln = c & 63, m = ln & 15, q = ln >> 4;
      if (c < 1792) {
        u32 h[8];
#pragma unroll
        for (int j = 0; j < 8; ++j) h[j] = lds_ld<unsigned short>(L, L_SCR + (((m >> 1) & 1) * 32 + 8 * q + j) * 464 + (8 * tau + 2 * (m >> 2) + (m & 1)) * 2);
        const int wv = tau / 7, n = ks * 7 + tau % 7;
        st16_sc1(rW, OFF_WFF + fs * WFF_FS + (u32)(n * 4 + wv) * 1024 + ln * 16, u32x4{h[0] | (h[1] << 16), h[2] | (h[3] << 16), h[4] | (h[5] << 16), h[6] | (h[7] << 16)}); } }
  } else if (kind == 1) {
    const int fs = (b - 112) / 7, k = (b - 112) % 7;
#pragma unroll
    for (int i = 0; i < 4; ++i) { const int c = tid + 512 * i, tau = c >> 6, ln = c & 63, m = ln & 15, q = ln >> 4, wv = tau / 7, tp = tau % 7;
      if (c < 1792) {
        const int col = 112 * wv + (tp < 6 ? 32 * (tp >> 1) + 8 * (m >> 2) + 4 * (tp & 1) + (m & 3) : 96 + m);
        u32 h[8];
#pragma unroll
        for (int j = 0; j < 8; ++j) h[j] = lds_ld<unsigned short>(L, L_SCR + (8 * q + j) * 912 + col * 2);
        const int n = tp < 4 ? 98 + k * 4 + tp : 126 + k * 3 + tp - 4;
        st16_sc1(rW, OFF_WFF + fs * WFF_FS + (u32)(n * 4 + wv) * 1024 + ln * 16, u32x4{h[0] | (h[1] << 16), h[2] | (h[3] << 16), h[4] | (h[5] << 16), h[6] | (h[7] << 16)}); } }
  } else if (kind == 2) {
    const int cc = (b - 168) / 7, tp = (b - 168) % 7;
#pragma unroll
    for (int i = 0; i < 4; ++i) { const int c = tid + 512 * i, fr = c >> 6, ln = c & 63, m = ln & 15, q = ln >> 4, T = fr >> 2, ec = fr & 3;
      if (c < 1792) {
        const u32 o = (u32)(m * 912 + (112 * ec + 16 * T + 4 * q) * 2);
        const u32x2 re = lds_ld<u32x2>(L, L_SCR + o), im = lds_ld<u32x2>(L, L_SCR + 16 * 912 + o);
        st16_sc1(rW, OFF_WH + cc * WH_CC + (u32)((T * 4 + ec) * 7 + tp) * 1024 + ln * 16, u32x4{re.x, re.y, im.x ^ 0x80008000u, im.y ^ 0x80008000u}); } }
  } else if (kind == 3) {
    if (tid < 64) {
      const int it0 = b - 196, m = tid & 1, c32 = tid >> 1;
      float s = 0.f;
#pragma unroll 8
      for (int rg = 0; rg < 64; ++rg) s += lds_ld<float>(L, L_SCR + (rg * 8 + m * 4 + (c32 >> 3)) * 32 + (c32 & 7) * 4);
      const int f = it0 * 32 + c32;
      __hip_atomic_store((GAS u32*)(p.ws + OFF_CS + (u32)(((f / 224) * 28 + (f % 224) / 8) * 16 + ((f & 7) >> 1) * 4 + m * 2 + (f & 1)) * 4), __builtin_bit_cast(u32, s), __ATOMIC_RELAXED, __HIP_MEMORY_SCOPE_AGENT);
    }
  }
  WAIT_V0(); BAR();
}

template <int NT> __device__ __forceinline__ void s_body(const Roles& R, LAS char* L, rsrc_t rW, int layer, int rg, int t0, int lane TR_ARG) {
  const int fr = lane & 15, fq = lane >> 4, rr = rg * 16 + fr;
  const u32 a0 = freshu(L_SA + (u32)(rr * 128 + ((fq ^ ((rr >> 1) & 7)) * 16))), a1 = freshu(L_SA + (u32)(rr * 128 + (((4 + fq) ^ ((rr >> 1) & 7)) * 16)));
  const u32 b0 = freshu(L_SB + (u32)(t0 * 1024 + lane * 16));
  f32x4 accP[NT], accQ[NT];
#pragma unroll
  for (int t = 0; t < NT; ++t) { accP[t] = f32x4{0, 0, 0, 0}; accQ[t] = f32x4{0, 0, 0, 0}; }
  bf16x8 ap[2][2], aq[2][2], bb[2][2][NT];
  auto rd = [&](int s, int n) {
    const u32 sa = (u32)(s % SA_N) * SA_ST, sb = (u32)(s % SB_N) * SB_ST;
    ap[n][0] = lds_ld<bf16x8>(L, a0 + sa); aq[n][0] = lds_ld<bf16x8>(L, a0 + sa + 8192);
#pragma unroll
    for (int t = 0; t < NT; ++t) bb[n][0][t] = lds_ld<bf16x8>(L, b0 + sb + t * 1024);
    ap[n][1] = lds_ld<bf16x8>(L, a1 + sa); aq[n][1] = lds_ld<bf16x8>(L, a1 + sa + 8192);
#pragma unroll
    for (int t = 0; t < NT; ++t) bb[n][1][t] = lds_ld<bf16x8>(L, b0 + sb + (7 + t) * 1024);
  };
  BAR(); BAR();
  rd(0, 0);
  sfor<0, 16>([&](auto sc) { constexpr int s = sc, c = s & 1;
    WAIT_L0(); BAR();
    if constexpr (s < 15) rd(s + 1, c ^ 1);
#pragma unroll
    for (int k = 0; k < 2; ++k)
#pragma unroll
      for (int t = 0; t < NT; ++t) { accP[t] = mfma(bb[c][k][t], ap[c][k], accP[t]); accQ[t] = mfma(bb[c][k][t], aq[c][k], accQ[t]); }
    if constexpr (s < 15) {
#pragma unroll
      for (int i = 0; i < 4 + 2 * NT; ++i) { __builtin_amdgcn_sched_group_barrier(0x8, 1, 0); __builtin_amdgcn_sched_group_barrier(0x100, 1, 0); }
      __builtin_amdgcn_sched_group_barrier(0x8, 2 * NT - 4, 0);
    }
    SCHED_FENCE();
  });
  const u32 o = OFF_PQ + layer * PQ_L + (u32)((R.s_kh * 128 + R.s_tt * 4 + rg) * 4 + R.s_ec) * 7168 + t0 * 1024 + lane * 16;
#pragma unroll
  for (int t = 0; t < NT; ++t)
    st16_sc1(rW, o + t * 1024, u32x4{pk_bf16(accP[t][0], accP[t][1]), pk_bf16(accP[t][2], accP[t][3]), pk_bf16(accQ[t][0], accQ[t][1]), pk_bf16(accQ[t][2], accQ[t][3])});
  WAIT_V0(); BAR();
}
__device__ __forceinline__ void s_comp(const Roles& R, LAS char* L, rsrc_t rW, int layer, int w, int lane TR_ARG) {
  if (w < 4) s_body<4>(R, L, rW, layer, w, 0, lane TR_PASS);
  else s_body<3>(R, L, rW, layer, w - 4, 4, lane TR_PASS);
}

__device__ __forceinline__ void h_comp(const Params& p, const Roles& R, LAS char* L, rsrc_t rW, int layer, int w, int lane TR_ARG) {
  const int tid = w * 64 + lane, ec = w & 3, i16 = w >> 2;
  f32x4 acc[7];
#pragma unroll
  for (int t = 0; t < 7; ++t) acc[t] = f32x4{0, 0, 0, 0};
  bf16x8 ww[2][7];
  const u32 l16 = (u32)lane * 16;
  const u32 wb = freshu(L_HW + (u32)ec * (HW_CAP * 1024) + l16);
  auto rdw = [&](int T, int n) {
#pragma unroll
    for (int t = 0; t < 7; ++t) ww[n][t] = lds_ld<bf16x8>(L, wb + ((7 * T + t) % HW_CAP) * 1024);
  };
  BAR();
  u32x4 aa[7][2];
  {
    const u32 ao = OFF_PQ + layer * PQ_L + (u32)(((2 * R.h_rt + i16) * 4 + ec) * 7) * 1024 + l16;
#pragma unroll
    for (int T = 0; T < 7; ++T)
#pragma unroll
      for (int kh = 0; kh < 2; ++kh) aa[T][kh] = ld16_sc1(rW, ao + (u32)(kh * 128 * 28 + T) * 1024);
  }
  const int g = tid & 15, rl = tid >> 4;
  const u32 ro = (u32)((32 * R.h_rt + rl) * DM + 112 * R.h_cc + 8 * (g < 14 ? g : 13)) * 2;
  u32x4 rs;
  if (layer == 0) rs = *(const GAS u32x4*)((const GAS char*)p.x0 + ro);
  else rs = ld16_sc1(rW, OFF_X2 + ro);
  SCHED_FENCE();
  BAR();
  rdw(0, 0);
  sfor<0, 7>([&](auto tc) { constexpr int T = tc, c = T & 1;
    WAIT_L0(); BAR();
    if constexpr (T < 6) rdw(T + 1, c ^ 1);
#pragma unroll
    for (int kh = 0; kh < 2; ++kh)
#pragma unroll
      for (int t = 0; t < 7; ++t) acc[t] = mfma(ww[c][t], __builtin_bit_cast(bf16x8, aa[T][kh]), acc[t]);
    if constexpr (T < 6) {
#pragma unroll
      for (int i = 0; i < 7; ++i) { __builtin_amdgcn_sched_group_barrier(0x8, 1, 0); __builtin_amdgcn_sched_group_barrier(0x100, 1, 0); }
      __builtin_amdgcn_sched_group_barrier(0x8, 7, 0);
    }
    SCHED_FENCE();
  });
#pragma unroll
  for (int t = 0; t < 7; ++t) lds_st<f32x4>(L, L_RED + (w * 7 + t) * 1024 + l16, acc[t]);
  WAIT_L0(); BAR();
  {
    const int gg = g < 14 ? g : 13, tp = gg >> 1, q0 = 2 * (gg & 1);
    f32x4 v0 = {0, 0, 0, 0}, v1 = {0, 0, 0, 0};
#pragma unroll
    for (int e = 0; e < 4; ++e) {
      const u32 o = L_RED + (u32)((((rl >> 4) * 4 + e) * 7 + tp) * 1024 + (q0 * 16 + (rl & 15)) * 16);
      v0 += lds_ld<f32x4>(L, o); v1 += lds_ld<f32x4>(L, o + 256);
    }
    u32x4 o4;
    o4.x = pk_bf16(bf_lo(rs.x) + v0[0], bf_hi(rs.x) + v0[1]); o4.y = pk_bf16(bf_lo(rs.y) + v0[2], bf_hi(rs.y) + v0[3]);
    o4.z = pk_bf16(bf_lo(rs.z) + v1[0], bf_hi(rs.z) + v1[1]); o4.w = pk_bf16(bf_lo(rs.w) + v1[2], bf_hi(rs.w) + v1[3]);
    const bool on = g < 14;
    const int row = 32 * R.h_rt + rl;
    if (on) st16_sc1(rW, OFF_X1R + layer * ACT_B + ro, o4);
    float x[8];
#pragma unroll
    for (int j = 0; j < 4; ++j) { x[2 * j] = bf_lo(o4[j]); x[2 * j + 1] = bf_hi(o4[j]); }
    float s = 0.f;
#pragma unroll
    for (int j = 0; j < 8; ++j) s += x[j];
    const float mc = sum16(on ? s : 0.f) * (1.f / 112.f);
    float q = 0.f;
#pragma unroll
    for (int j = 0; j < 8; ++j) { const float d = x[j] - mc; q += d * d; }
    const float m2 = sum16(on ? q : 0.f);
    if (g == 0) st8_sc1(rW, OFF_STATS + layer * STATS_L + (u32)(row * 4 + R.h_cc) * 8, u32x2{__builtin_bit_cast(u32, mc), __builtin_bit_cast(u32, m2)});
  }
  WAIT_V0(); BAR();
}

__device__ __forceinline__ void gd_comp(const Roles& R, LAS char* L, rsrc_t rW, int layer, int w, int lane TR_ARG) {
  const int fr = lane & 15, fq = lane >> 4, c4 = w & 3, r = w >> 2;
  const u32 l16 = (u32)lane * 16;
  f32x4 acc[2][7];
  bf16x8 xx[2][4], ww[2][8];
  const u32 wb = freshu(L_GW + (u32)c4 * (GW_CAP * 1024) + l16);
  auto rdw = [&](auto sc, int n) {
    constexpr int sg = sc;
#pragma unroll
    for (int t = 0; t < gd_end(sg) - gd_begin(sg); ++t) ww[n][t] = lds_ld<bf16x8>(L, wb + ((gd_begin(sg) + t) % GW_CAP) * 1024);
  };
  const u32 xb0 = freshu(L_GX + (u32)((32 * r + fr) * 128 + ((fq ^ ((fr >> 1) & 7)) * 16))), xb1 = freshu(L_GX + (u32)((32 * r + fr) * 128 + (((4 + fq) ^ ((fr >> 1) & 7)) * 16)));
  auto rdx = [&](int ks, int n) {
#pragma unroll
    for (int i = 0; i < 2; ++i) xx[n][i] = lds_ld<bf16x8>(L, ((ks & 1) ? xb1 : xb0) + (ks >> 1) * 8192 + i * 2048);
  };
  const u32 hr = freshu(L_GX + (u32)r * 2048 + l16), hw = freshu(L_GX + (u32)r * 2048 + l16 + c4 * 4);
  auto rdh = [&](int k, int n, int half) {
#pragma unroll
    for (int i = 0; i < 2; ++i) xx[n][half * 2 + i] = lds_ld<bf16x8>(L, hr + (u32)(k * 4 + i) * 1024);
  };
  BAR();
  float var[2], c1[2];
  {
    u32x4 st[2][2];
#pragma unroll
    for (int i = 0; i < 2; ++i) {
      const u32 o = OFF_STATS + layer * STATS_L + (u32)(64 * R.g_rb + 32 * r + 16 * i + fr) * 32;
      st[i][0] = ld16_sc1(rW, o); st[i][1] = ld16_sc1(rW, o + 16);
    }
    f32x4 cs[7];
#pragma unroll
    for (int t = 0; t < 7; ++t) cs[t] = __builtin_bit_cast(f32x4, ld16_sc1(rW, OFF_CS + (u32)((R.g_fs * 28 + c4 * 7 + t) * 16 + 4 * fq) * 4));
#pragma unroll
    for (int i = 0; i < 2; ++i) {
      const float m0 = as_f(st[i][0].x), m1 = as_f(st[i][0].z), m2 = as_f(st[i][1].x), m3 = as_f(st[i][1].z);
      const float mean = 0.25f * ((m0 + m1) + (m2 + m3));
      const float d0 = m0 - mean, d1 = m1 - mean, d2 = m2 - mean, d3 = m3 - mean;
      const float M2 = (as_f(st[i][0].y) + as_f(st[i][0].w)) + (as_f(st[i][1].y) + as_f(st[i][1].w)) + 112.f * ((d0 * d0 + d1 * d1) + (d2 * d2 + d3 * d3));
      var[i] = M2 * (1.f / DM) + LN_EPS;
      c1[i] = -1.4426950408889634f * __builtin_amdgcn_rsqf(var[i]);
#pragma unroll
      for (int t = 0; t < 7; ++t) acc[i][t] = cs[t] * (-mean);
    }
  }
  SCHED_FENCE();
  BAR();
  rdx(0, 0);
  rdw(std::integral_constant<int, 0>{}, 0);
  sfor<0, 14>([&](auto jc) { constexpr int j = jc, c = j & 1;
    WAIT_L0(); BAR();
    if constexpr (j < 13) { rdx(j + 1, c ^ 1); rdw(std::integral_constant<int, j + 1>{}, c ^ 1); }
#pragma unroll
    for (int t = 0; t < 7; ++t)
#pragma unroll
      for (int i = 0; i < 2; ++i) acc[i][t] = mfma(ww[c][t], xx[c][i], acc[i][t]);
    if constexpr (j < 13) {
#pragma unroll
      for (int i = 0; i < 9; ++i) { __builtin_amdgcn_sched_group_barrier(0x8, 1, 0); __builtin_amdgcn_sched_group_barrier(0x100, 1, 0); }
      __builtin_amdgcn_sched_group_barrier(0x8, 5, 0);
    }
    SCHED_FENCE();
  });
#pragma unroll
  for (int t = 0; t < 7; ++t)
#pragma unroll
    for (int i = 0; i < 2; ++i) {
      const f32x4 a = acc[i][t];
      const f32x2 gg = {a[0], a[1]}, uu = {a[2], a[3]};
      const f32x2 z = gg * c1[i];
      const f32x2 e = {__builtin_amdgcn_exp2f(z[0]), __builtin_amdgcn_exp2f(z[1])};
      const f32x2 dn = e * var[i] + var[i];
      const f32x2 rc = {__builtin_amdgcn_rcpf(dn[0]), __builtin_amdgcn_rcpf(dn[1])};
      const f32x2 h = (gg * uu) * rc;
      lds_st<u32>(L, hw + (u32)(t * 4 + i) * 1024, pk_bf16(h[0], h[1]));
    }
  WAIT_L0(); BAR();
  const u32 po = OFF_PART + layer * PART_L + (u32)R.g_fs * PART_FS + (u32)((4 * R.g_rb + 2 * r) * 4 + c4) * 4096;
  auto put = [&](const f32x4 lo, const f32x4 hi, int i, int pp) {
    st16_sc1(rW, po + (u32)i * 16384 + pp * 1024 + l16, u32x4{pk_bf16(lo[0], lo[1]), pk_bf16(lo[2], lo[3]), pk_bf16(hi[0], hi[1]), pk_bf16(hi[2], hi[3])});
  };
  f32x4 aA[2][4], aB[2][3];
#pragma unroll
  for (int t = 0; t < 8; ++t) aA[t >> 2][t & 3] = f32x4{0, 0, 0, 0};
#pragma unroll
  for (int t = 0; t < 6; ++t) aB[t / 3][t % 3] = f32x4{0, 0, 0, 0};
  rdh(0, 0, 0); rdh(1, 0, 1);
  rdw(std::integral_constant<int, 14>{}, 0);
  sfor<14, GD_NST>([&](auto sc) {
    constexpr int sg = sc, c = sg & 1;
    constexpr bool second = sg >= 18;
    constexpr int j = second ? sg - 18 : sg - 14, NT = second ? 3 : 4;
    constexpr bool two = j < 3;
    WAIT_L0(); BAR();
    if constexpr (sg + 1 < GD_NST) {
      constexpr int nj = (sg + 1 >= 18) ? sg + 1 - 18 : sg + 1 - 14;
      rdh(2 * nj, c ^ 1, 0);
      if constexpr (nj < 3) rdh(2 * nj + 1, c ^ 1, 1);
      rdw(std::integral_constant<int, sg + 1>{}, c ^ 1);
    }
#pragma unroll
    for (int half = 0; half < (two ? 2 : 1); ++half)
#pragma unroll
      for (int t = 0; t < NT; ++t)
#pragma unroll
        for (int i = 0; i < 2; ++i) {
          if constexpr (second) aB[i][t] = mfma(ww[c][half * NT + t], xx[c][half * 2 + i], aB[i][t]);
          else aA[i][t] = mfma(ww[c][half * NT + t], xx[c][half * 2 + i], aA[i][t]);
        }
    if constexpr (second && j < 2) { put(aA[j][0], aA[j][1], j, 0); put(aA[j][2], aA[j][3], j, 1); }
    {
      constexpr int nm = (two ? 2 : 1) * NT * 2;
      constexpr int nrd = sg + 1 < GD_NST ? gd_end(sg + 1) - gd_begin(sg + 1) + (((sg + 1 >= 18) ? sg + 1 - 18 : sg + 1 - 14) < 3 ? 4 : 2) : 0;
#pragma unroll
      for (int i = 0; i < nm; ++i) {
        __builtin_amdgcn_sched_group_barrier(0x8, 1, 0);
        if (i < nrd) __builtin_amdgcn_sched_group_barrier(0x100, 1, 0);
        if (second && j < 2) __builtin_amdgcn_sched_group_barrier(0x2 | 0x40, 1, 0);
      }
    }
    SCHED_FENCE();
  });
#pragma unroll
  for (int i = 0; i < 2; ++i) {
    put(aB[i][0], aB[i][1], i, 2);
    const u32 d0 = pk_bf16(aB[i][2][0], aB[i][2][1]), d1 = pk_bf16(aB[i][2][2], aB[i][2][3]);
    const auto s0 = __builtin_amdgcn_permlane16_swap(d0, d0, false, false), s1 = __builtin_amdgcn_permlane16_swap(d1, d1, false, false);
    if ((fq & 1) == 0) st16_sc1(rW, po + (u32)i * 16384 + 3072 + (u32)((fq >> 1) * 16 + fr) * 16, u32x4{s0[0], s1[0], s0[1], s1[1]});
  }
  WAIT_V0(); BAR();
}

__device__ __forceinline__ void r_comp(const Params& p, const Roles& R, LAS char* L, rsrc_t rW, int layer, int w, int lane TR_ARG) {
  const int b = R.b, tid = w * 64 + lane, r8 = lane & 7, sl = lane >> 3, c4 = w & 3, pc = w >> 2;
  const bool on = pc == 0 || sl < 6;
  const int s1 = on ? sl : 5, blk = 2 * pc + (s1 >> 2), qq = s1 & 3, col = 112 * c4 + 32 * blk + 8 * qq;
  const float ncol = pc ? 48.f : 64.f;
  BAR();
  u32x4 v[9];
  const u32 po = OFF_PART + layer * PART_L + (u32)(((b >> 1) * 4 + c4) * 4 + blk) * 1024 + (u32)(qq * 16 + (b & 1) * 8 + r8) * 16;
#pragma unroll
  for (int f = 0; f < 8; ++f) v[f] = ld16_sc1(rW, po + f * PART_FS);
  const u32 ro = (u32)((8 * b + r8) * DM + col) * 2;
  v[8] = ld16_sc1(rW, OFF_X1R + layer * ACT_B + ro);
  float y[8] = {0, 0, 0, 0, 0, 0, 0, 0};
#pragma unroll
  for (int f = 0; f < 8; ++f)
#pragma unroll
    for (int j = 0; j < 4; ++j) { y[2 * j] += bf_lo(v[f][j]); y[2 * j + 1] += bf_hi(v[f][j]); }
  u32x4 o;
#pragma unroll
  for (int j = 0; j < 4; ++j) o[j] = pk_bf16(bf_lo(v[8][j]) + y[2 * j], bf_hi(v[8][j]) + y[2 * j + 1]);
  if (on) {
    if (layer == 0) st16_sc1(rW, OFF_X2 + ro, o);
    else *(GAS u32x4*)((GAS char*)p.out + ro) = o;
  }
  float x[8];
#pragma unroll
  for (int j = 0; j < 4; ++j) { x[2 * j] = bf_lo(o[j]); x[2 * j + 1] = bf_hi(o[j]); }
  auto rowsum = [&](float s) {
    s = dpp_add<0x128>(s);
    const auto a = __builtin_amdgcn_permlane16_swap(__builtin_bit_cast(u32, s), __builtin_bit_cast(u32, s), false, false);
    s = as_f(a[0]) + as_f(a[1]);
    const auto c = __builtin_amdgcn_permlane32_swap(__builtin_bit_cast(u32, s), __builtin_bit_cast(u32, s), false, false);
    return as_f(c[0]) + as_f(c[1]);
  };
  if (layer == 0) {
    float s = 0.f;
#pragma unroll
    for (int j = 0; j < 8; ++j) s += x[j];
    const float mw = rowsum(on ? s : 0.f) / ncol;
    float q = 0.f;
#pragma unroll
    for (int j = 0; j < 8; ++j) { const float d = x[j] - mw; q += d * d; }
    const float m2 = rowsum(on ? q : 0.f);
    if (sl == 0) lds_st<f32x2>(L, L_MISC + (u32)(r8 * 8 + w) * 8, f32x2{mw, m2});
  }
  WAIT_L0(); BAR();
  if (layer == 0) {
    f32x4 s[4];
#pragma unroll
    for (int i = 0; i < 4; ++i) s[i] = lds_ld<f32x4>(L, L_MISC + r8 * 64 + i * 16);
    const float mean = (64.f * ((s[0][0] + s[0][2]) + (s[1][0] + s[1][2])) + 48.f * ((s[2][0] + s[2][2]) + (s[3][0] + s[3][2]))) * (1.f / DM);
    float M2 = 0.f;
#pragma unroll
    for (int i = 0; i < 4; ++i) { const float n = i < 2 ? 64.f : 48.f, d0 = s[i][0] - mean, d1 = s[i][2] - mean; M2 += (s[i][1] + s[i][3]) + n * (d0 * d0 + d1 * d1); }
    const float rstd = __builtin_amdgcn_rsqf(M2 * (1.f / DM) + LN_EPS);
    u32x4 xo;
#pragma unroll
    for (int j = 0; j < 4; ++j) xo[j] = pk_bf16((x[2 * j] - mean) * rstd, (x[2 * j + 1] - mean) * rstd);
    if (on) lds_st<u32x4>(L, L_T + r8 * T_PITCH + col * 2, xo);
  }
  WAIT_L0(); BAR();
  if (layer == 0) xn_store(L, rW, OFF_XN + ACT_B, b, tid);
  WAIT_V0(); BAR();
}

__global__ __launch_bounds__(NTHR, 3) void fnet_swiglu_persistent(Params p) {
  extern __shared__ __attribute__((aligned(16))) char smem[];
  LAS char* L = (LAS char*)smem;
  const int w = __builtin_amdgcn_readfirstlane(threadIdx.x >> 6), lane = threadIdx.x & 63;
  const Roles R(blockIdx.x);
  if (w < 8) {
    const rsrc_t rW = __builtin_amdgcn_make_buffer_rsrc((void*)p.ws, 0, WS_USED, 0x00020000);
    p0_comp(p, R, L, rW, w, lane TR_PASS);
#pragma unroll 1
    for (int layer = 0; layer < 2; ++layer) {
      s_comp(R, L, rW, layer, w, fresh(lane) TR_PASS);
      h_comp(p, R, L, rW, layer, w, fresh(lane) TR_PASS);
      gd_comp(R, L, rW, layer, w, fresh(lane) TR_PASS);
      r_comp(p, R, L, rW, layer, w, fresh(lane) TR_PASS);
    }
  } else if (w == 9 || w == 10) {
    loader_role(p, R, w - 9, lane TR_PASS);
  } else {
    sync_role(p, R, w == 8, lane TR_PASS);
  }
}

}

extern "C" void kernel_launch(void* const* d_in, const int* in_sizes, int n_in, void* d_out, int out_size, void* d_ws, size_t ws_size,
                              hipStream_t stream) {
  (void)in_sizes; (void)n_in; (void)out_size; (void)ws_size;
  fk::Params p;
  p.x0 = d_in[0]; p.Cr = d_in[1]; p.Ci = d_in[2]; p.Wr = d_in[3]; p.Wi = d_in[4];
  p.Wg = d_in[6]; p.Wu = d_in[7]; p.Wd = d_in[8];
  p.out = d_out; p.ws = (char*)d_ws;
  (void)hipFuncSetAttribute((const void*)fk::fnet_swiglu_persistent, hipFuncAttributeMaxDynamicSharedMemorySize, fk::LDS_BYTES);
  fk::fnet_swiglu_persistent<<<fk::NWG, fk::NTHR, fk::LDS_BYTES, stream>>>(p);
}
```

```cpp
#include <hip/hip_runtime.h>
#include <hip/hip_bf16.h>
#include <stdint.h>
#include <type_traits>

namespace fk {

#define LAS __attribute__((address_space(3)))
#define GAS __attribute__((address_space(1)))
typedef unsigned u32;
typedef unsigned long long u64;
typedef u32 u32x4 __attribute__((ext_vector_type(4)));
typedef u32 u32x2 __attribute__((ext_vector_type(2)));
typedef int i32x4 __attribute__((ext_vector_type(4)));
typedef short bf16x8 __attribute__((ext_vector_type(8)));
typedef float f32x4 __attribute__((ext_vector_type(4)));
typedef float f32x2 __attribute__((ext_vector_type(2)));

constexpr int SEQ = 2048, DM = 448, FF = 1792;
constexpr int NWG = 256, NTHR = 768;
constexpr float LN_EPS = 1e-5f;

constexpr u32 OFF_SALT = 0, OFF_ABORT = 256;
constexpr u32 OFF_FP0A = 4096, OFF_FP0B = 8192, OFF_FR = 12288;
constexpr u32 OFF_FS = 16384, OFF_FH = 24576, OFF_FG = 32768;
constexpr u32 OFF_CS = 65536;
constexpr u32 OFF_STATS = OFF_CS + 32768;
constexpr u32 STATS_L = SEQ * 4 * 8;
constexpr u32 OFF_WH = OFF_STATS + 2 * STATS_L;
constexpr u32 WH_CC = 196 * 1024;
constexpr u32 OFF_WFF = OFF_WH + 4 * WH_CC;
constexpr u32 WFF_FS = 588 * 1024;
constexpr u32 ACT_B = SEQ * DM * 2;
constexpr u32 OFF_XN = OFF_WFF + 8 * WFF_FS;
constexpr u32 OFF_PQ = OFF_XN + 2 * ACT_B;
constexpr u32 PQ_L = 4 * ACT_B;
constexpr u32 OFF_X1R = OFF_PQ + 2 * PQ_L;
constexpr u32 OFF_PART = OFF_X1R + 2 * ACT_B;
constexpr u32 PART_FS = 128 * 16 * 1024, PART_L = 8 * PART_FS;
constexpr u32 OFF_X2 = OFF_PART + 2 * PART_L;
constexpr u32 WS_USED = OFF_X2 + ACT_B;

#define FK_SA_N 5
#define FK_SB_N 5
constexpr u32 L_SA = 0, SA_ST = 16384;            constexpr int SA_N = FK_SA_N;
constexpr u32 L_SB = SA_N * SA_ST, SB_ST = 14336; constexpr int SB_N = FK_SB_N;
constexpr u32 L_HW = 0;                           constexpr int HW_CAP = 39;
constexpr u32 L_RED = 102400;
constexpr u32 L_GW = 0;                           constexpr int GW_CAP = 25;
constexpr u32 L_GX = 102400;
constexpr u32 L_SCR = L_SB;
constexpr u32 L_T = L_SCR + 30720;  constexpr u32 T_PITCH = 912;
constexpr u32 L_MISC = 159744;
constexpr u32 LDS_BYTES = 163840;
static_assert(L_SB + SB_N * SB_ST <= L_MISC && SA_N * SA_ST <= L_SCR && L_T + 8 * T_PITCH <= L_MISC && 4 * HW_CAP * 1024 <= L_MISC && 4 * GW_CAP * 1024 <= L_GX, "LDS map");

struct Params {
  const void* x0; const void* Cr; const void* Ci; const void* Wr; const void* Wi;
  const void* Wg; const void* Wu; const void* Wd; void* out; char* ws;
};

template <int LO, int HI, class F> __device__ __forceinline__ void sfor(F&& f) {
  if constexpr (HI - LO == 1) f(std::integral_constant<int, LO>{});
  else if constexpr (HI - LO > 1) { sfor<LO, (LO + HI) / 2>(f); sfor<(LO + HI) / 2, HI>(f); }
}
#define BAR() asm volatile("s_barrier" ::: "memory")
#define TR_ARG
#define TR_ARG0
#define TR_PASS
#define TR_PASS0
#define WAIT_L0() asm volatile("s_waitcnt lgkmcnt(0)" ::: "memory")
#define WAIT_V0() asm volatile("s_waitcnt vmcnt(0)" ::: "memory")
template <int N> __device__ __forceinline__ void wait_v() { asm volatile("s_waitcnt vmcnt(%0)" ::"n"(N > 63 ? 63 : N) : "memory"); }
#define SCHED_FENCE() __builtin_amdgcn_sched_barrier(0)
constexpr int AUX_SC1 = 16;

__device__ __forceinline__ i32x4 make_rsrc(const void* p, u32 bytes) {
  const u64 a = (u64)p;
  i32x4 r;
  r.x = __builtin_amdgcn_readfirstlane((int)(u32)a);
  r.y = __builtin_amdgcn_readfirstlane((int)(u32)(a >> 32) & 0xffff);
  r.z = (int)bytes;
  r.w = 0x00020000;
  return r;
}
template <bool SC1, u32 LIMM, u32 SIMM> __device__ __forceinline__ void dma16(u32 lbase, u32 voff, i32x4 rs) {
  u32 tmp;
  if constexpr (SC1) asm volatile("s_add_u32 m0, %1, %4\n\ts_mov_b32 %0, %5\n\tbuffer_load_dwordx4 %2, %3, %0 offen sc1 lds" : "=&s"(tmp) : "s"(lbase), "v"(voff), "s"(rs), "n"(LIMM), "n"(SIMM) : "scc");
  else asm volatile("s_add_u32 m0, %1, %4\n\ts_mov_b32 %0, %5\n\tbuffer_load_dwordx4 %2, %3, %0 offen lds" : "=&s"(tmp) : "s"(lbase), "v"(voff), "s"(rs), "n"(LIMM), "n"(SIMM) : "scc");
}
template <bool SC1, u32 LIMM, u32 SIMM> __device__ __forceinline__ void dma16s(u32 lbase, u32 voff, i32x4 rs, u32 sdyn) {
  u32 tmp;
  if constexpr (SC1) asm volatile("s_add_u32 m0, %1, %5\n\ts_add_u32 %0, %4, %6\n\tbuffer_load_dwordx4 %2, %3, %0 offen sc1 lds" : "=&s"(tmp) : "s"(lbase), "v"(voff), "s"(rs), "s"(sdyn), "n"(LIMM), "n"(SIMM) : "scc");
  else asm volatile("s_add_u32 m0, %1, %5\n\ts_add_u32 %0, %4, %6\n\tbuffer_load_dwordx4 %2, %3, %0 offen lds" : "=&s"(tmp) : "s"(lbase), "v"(voff), "s"(rs), "s"(sdyn), "n"(LIMM), "n"(SIMM) : "scc");
}
__device__ __forceinline__ u32 freshs(u32 v) { asm volatile("" : "+s"(v)); return v; }
__device__ __forceinline__ u32 pk_bf16(float lo, float hi) {
  typedef __bf16 bf2 __attribute__((ext_vector_type(2)));
  bf2 r; r.x = (__bf16)lo; r.y = (__bf16)hi;
  return __builtin_bit_cast(u32, r);
}
__device__ __forceinline__ float as_f(u32 v) { return __builtin_bit_cast(float, v); }
__device__ __forceinline__ float bf_lo(u32 v) { return __builtin_bit_cast(float, v << 16); }
__device__ __forceinline__ float bf_hi(u32 v) { return __builtin_bit_cast(float, v & 0xffff0000u); }
__device__ __forceinline__ f32x4 mfma(bf16x8 a, bf16x8 b, f32x4 c) { return __builtin_amdgcn_mfma_f32_16x16x32_bf16(a, b, c, 0, 0, 0); }
template <int CTRL> __device__ __forceinline__ float dpp_add(float x) {
  return x + __builtin_bit_cast(float, __builtin_amdgcn_update_dpp(0, __builtin_bit_cast(int, x), CTRL, 0xf, 0xf, true));
}
__device__ __forceinline__ float sum16(float x) {
  x = dpp_add<0xB1>(x);
  x = dpp_add<0x4E>(x);
  x = dpp_add<0x141>(x);
  x = dpp_add<0x140>(x);
  return x;
}
__device__ __forceinline__ float sum64(float x) {
  x = sum16(x);
  const int xi = __builtin_bit_cast(int, x);
  const float a = __builtin_bit_cast(float, __builtin_amdgcn_readlane(xi, 0)), b = __builtin_bit_cast(float, __builtin_amdgcn_readlane(xi, 16));
  const float c = __builtin_bit_cast(float, __builtin_amdgcn_readlane(xi, 32)), d = __builtin_bit_cast(float, __builtin_amdgcn_readlane(xi, 48));
  return (a + b) + (c + d);
}
__device__ __forceinline__ int fresh(int v) { asm volatile("" : "+v"(v)); return v; }
__device__ __forceinline__ u32 freshu(u32 v) { asm volatile("" : "+v"(v)); return v; }
typedef __amdgpu_buffer_rsrc_t rsrc_t;
__device__ __forceinline__ u32x4 ld16_sc1(rsrc_t r, u32 off) { return __builtin_amdgcn_raw_buffer_load_b128(r, (int)off, 0, AUX_SC1); }
__device__ __forceinline__ void st16_sc1(rsrc_t r, u32 off, u32x4 v) { __builtin_amdgcn_raw_buffer_store_b128(v, r, (int)off, 0, AUX_SC1); }
__device__ __forceinline__ void st8_sc1(rsrc_t r, u32 off, u32x2 v) { __builtin_amdgcn_raw_buffer_store_b64(v, r, (int)off, 0, AUX_SC1); }
template <class T> __device__ __forceinline__ T lds_ld(LAS char* l, u32 off) { return *(const LAS T*)(l + off); }
template <class T> __device__ __forceinline__ void lds_st(LAS char* l, u32 off, T v) { *(LAS T*)(l + off) = v; }

struct Roles {
  int b, s_kh, s_tt, s_ec, h_rt, h_cc, g_rb, g_fs;
  __device__ __forceinline__ explicit Roles(int b_) {
    b = b_;
    const int x = b & 7, j = b >> 3;
    s_kh = x & 1; s_tt = (x >> 1) * 8 + (j >> 2); s_ec = j & 3;
    h_rt = x * 8 + (j >> 2); h_cc = j & 3;
    g_fs = (x & 3) * 2 + (j & 1); g_rb = (x >> 2) * 16 + (j >> 1);
  }
};

#define FK_ROT(tt) (((tt) * 5) & 15)
#define FK_P0_PRE 2
enum : int { OP_BAR, OP_WAIT, OP_WAITL, OP_SA, OP_SB, OP_HW, OP_GX, OP_GW, OP_LSA, OP_WSA, OP_LGW, OP_WGW, OP_TOUCH, OP_SLEEP, OP_SINK };
struct Op { int kind, a, b, c, d; };
constexpr int PROG_MAX = 2600;
struct Prog { Op op[PROG_MAX]; int n; };
constexpr int imax(int a, int b) { return a > b ? a : b; }
constexpr int imin(int a, int b) { return a < b ? a : b; }
struct Builder {
  Prog p{}; int issued = 0, landed = -1; bool wrote = false;
  constexpr void push(int k, int a = 0, int b = 0, int c = 0, int d = 0) { p.op[p.n] = Op{k, a, b, c, d}; ++p.n; }
  constexpr int vm(int k, int a, int b = 0, int c = 0, int d = 0) { push(k, a, b, c, d); return issued++; }
  constexpr void wait_for(int pos) { if (pos > landed) { push(OP_WAIT, issued - 1 - pos); landed = pos; } }
  constexpr void lds_write(int k, int a, int b, int c, int d) { push(k, a, b, c, d); wrote = true; }
  constexpr void bar() { if (wrote) { push(OP_WAITL); wrote = false; } push(OP_BAR); }
};
constexpr int S_BARS = 19, H_BARS = 11, G_BARS = 26, R_BARS = 4, P0_BARS = 3;
constexpr int S_TILED_AT = 3, S_TILED_POLL = 8;
constexpr int GD_NST = 22;
constexpr int gd_end(int sg) { return sg < 14 ? 7 * (sg + 1) : sg < 17 ? 98 + 8 * (sg - 13) : sg == 17 ? 126 : sg < 21 ? 126 + 6 * (sg - 17) : 147; }
constexpr int gd_begin(int sg) { return sg == 0 ? 0 : gd_end(sg - 1); }
constexpr int hw_slot(int c, int n) { return (n + ((c & 1) ? 0 : 16)) % HW_CAP; }

#define FK_HW_FROM 12
#define FK_HW_TO 13
#define FK_GW_FROM 2
#define FK_GW_TO 4
#define FK_SAP_AT 2
#define FK_STAGE 0
#define FK_GST_AT 4
#define FK_TOUCH_AT -1
#define FK_TOUCH_SLEEP 0
#define FK_PRE_SLEEP 0
constexpr int STG = 32;
constexpr int SA_ST0 = FK_STAGE ? 4 : 0, SA_ST1 = FK_STAGE ? 2 : 0;
constexpr int GW_ST0 = FK_STAGE ? 16 : 0, GW_ST1 = FK_STAGE ? 8 : 0;

constexpr Prog build_prog(int role) {
  Builder B{};
  auto need = [&](const int* pos, int lo, int hi) { int m = -1; for (int n = lo; n < hi; ++n) m = imax(m, pos[n]); return m; };
  int posSA[16] = {}, posST[STG] = {};
  auto sa_prefetch = [&]() {
    for (int s = 0; s < 16; ++s) posSA[s] = -1;
    if (role == 0) {
      for (int s = 0; s < SA_N; ++s) for (int i = 0; i < 8; ++i) posSA[s] = B.vm(OP_SA, s, i);
      for (int k = 0; k < SA_ST0 * 8; ++k) posST[k] = B.vm(OP_LSA, SA_N + k / 8, k % 8, -1, k);
    } else for (int k = 0; k < SA_ST1 * 16; ++k) posST[k] = B.vm(OP_LSA, SA_N + SA_ST0 + k / 16, k % 8, (k / 8) & 1, k);
  };
  auto touches = [&]() { if (role == 1) { for (int i = 0; i < FK_TOUCH_SLEEP; ++i) B.push(OP_SLEEP); for (int i = 0; i < 16; ++i) B.vm(OP_TOUCH, i >> 1, i & 1); } };
  for (int i = 0; i <= P0_BARS; ++i) {
    if (i == FK_P0_PRE) { if (role == 0) for (int k = 0; k < FK_PRE_SLEEP; ++k) B.push(OP_SLEEP); sa_prefetch(); }
    if (i == FK_TOUCH_AT) touches();
    if (i < P0_BARS) B.bar();
  }
  for (int layer = 0; layer < 2; ++layer) {
    int posSB[16] = {};
    bool busy[160] = {};
    int posHW[49] = {}; bool doneHW[49][2] = {}; int nHW = 0;
    int posGW[147] = {}; bool doneGW[147][2] = {}; int nGW = 0;
    for (int n = 0; n < 49; ++n) posHW[n] = -1;
    for (int n = 0; n < 147; ++n) posGW[n] = -1;
    for (int n = 0; n < 16; ++n) posSB[n] = -1;
    auto hw_pre = [&]() { if (role == 0) for (int n = 0; n < HW_CAP; ++n) for (int ws = 0; ws < 2; ++ws)
        if (!doneHW[n][ws] && !busy[ws * HW_CAP + hw_slot(ws, n)] && !busy[(2 + ws) * HW_CAP + hw_slot(ws, n)]) { posHW[n] = B.vm(OP_HW, n, ws); doneHW[n][ws] = true; } };
    auto hw_upto = [&](int lim) { for (; nHW < imin(49, lim); ++nHW) for (int ws = 0; ws < 2; ++ws)
        if (role == 0 && !doneHW[nHW][ws]) { posHW[nHW] = B.vm(OP_HW, nHW, ws); doneHW[nHW][ws] = true; } };
    auto gw_pre = [&]() { if (role == 0) for (int n = 0; n < GW_CAP; ++n) for (int ws = 0; ws < 2; ++ws)
        if (!doneGW[n][ws] && !busy[ws * GW_CAP + n] && !busy[(2 + ws) * GW_CAP + n]) { posGW[n] = B.vm(OP_GW, n, ws); doneGW[n][ws] = true; } };
    auto gw_stage = [&]() {
      if (role == 0) for (int k = 0; k < GW_ST0 * 2; ++k) posST[k] = B.vm(OP_LGW, GW_CAP + k / 2, k & 1, 0, k);
      else for (int k = 0; k < GW_ST1 * 4; ++k) posST[k] = B.vm(OP_LGW, GW_CAP + GW_ST0 + k / 4, k & 3, 0, k);
    };
    auto gw_upto = [&](int lim) {
      for (; nGW < imin(147, lim); ++nGW) {
        const int n = nGW;
        if (n >= GW_CAP && n < GW_CAP + GW_ST0) {
          if (role == 0) for (int ws = 0; ws < 2; ++ws) { const int k = (n - GW_CAP) * 2 + ws; B.wait_for(posST[k]); B.lds_write(OP_WGW, n, ws, 0, k); }
        } else if (n >= GW_CAP + GW_ST0 && n < GW_CAP + GW_ST0 + GW_ST1) {
          if (role == 1) for (int c = 0; c < 4; ++c) { const int k = (n - GW_CAP - GW_ST0) * 4 + c; B.wait_for(posST[k]); B.lds_write(OP_WGW, n, c, 0, k); }
        } else if (role == 0) for (int ws = 0; ws < 2; ++ws) if (!doneGW[n][ws]) { posGW[n] = B.vm(OP_GW, n, ws); doneGW[n][ws] = true; }
      } };
    for (int k = 0; k < 160; ++k) busy[k] = k < (int)(L_SB + SB_N * SB_ST) / 1024;
    B.bar();
    if (role == 0) for (int s = 0; s < SB_N; ++s) for (int i = 0; i < 7; ++i) posSB[s] = B.vm(OP_SB, s, i, layer);
    B.wait_for(imax(posSA[0], posSB[0])); B.bar();
    for (int s = 0; s < 16; ++s) {
      if (s < 15) B.wait_for(imax(posSA[s + 1], posSB[s + 1]));
      B.bar();
      if (s + SB_N < 16) { if (role == 0) for (int i = 0; i < 7; ++i) posSB[s + SB_N] = B.vm(OP_SB, s + SB_N, i, layer); }
      else for (int k = 0; k < 14; ++k) busy[L_SB / 1024 + (s % SB_N) * 14 + k] = false;
      const int t = s + SA_N;
      if (t < SA_N + SA_ST0) { if (role == 0) for (int i = 0; i < 8; ++i) { const int k = (t - SA_N) * 8 + i; B.wait_for(posST[k]); B.lds_write(OP_WSA, t, i, -1, k); } }
      else if (t < SA_N + SA_ST0 + SA_ST1) { if (role == 1) for (int i = 0; i < 16; ++i) { const int k = (t - SA_N - SA_ST0) * 16 + i; B.wait_for(posST[k]); B.lds_write(OP_WSA, t, i & 7, i >> 3, k); } }
      else if (t < 16) { if (role == 0) for (int i = 0; i < 8; ++i) posSA[t] = B.vm(OP_SA, t, i); }
      else for (int k = 0; k < 16; ++k) busy[(s % SA_N) * 16 + k] = false;
      if (s == 15) for (int k = 0; k < 160; ++k) busy[k] = false;
      if (s >= FK_HW_FROM && s <= FK_HW_TO) hw_pre();
    }
    B.bar();
    if (role == 1 && layer == 0 && FK_TOUCH_AT >= 0) { B.wait_for(B.issued - 1); B.push(OP_SINK); }
    hw_upto(HW_CAP);
    for (int k = 0; k < 160; ++k) busy[k] = true;
    B.bar();
    B.wait_for(need(posHW, 0, 7)); B.bar();
    for (int t = 0; t < 7; ++t) {
      if (t < 6) B.wait_for(need(posHW, 7 * (t + 1), 7 * (t + 2)));
      B.bar();
      hw_upto(7 * (t + 1) + HW_CAP);
      for (int n = 7 * t; n < 7 * t + 7; ++n) if (n + HW_CAP >= 49) for (int c = 0; c < 4; ++c) busy[c * HW_CAP + hw_slot(c, n)] = false;
      if (t >= FK_GW_FROM && t <= FK_GW_TO) gw_pre();
      if (t == FK_GST_AT) gw_stage();
    }
    B.bar();
    if (FK_GST_AT == 7) gw_stage();
    B.bar();
    gw_upto(GW_CAP);
    if (FK_GST_AT >= 8) gw_stage();
    B.bar();
    int posGX[7] = {-1, -1, -1, -1, -1, -1, -1};
    if (role == 0) for (int i = 0; i < 28; ++i) posGX[i / 4] = B.vm(OP_GX, i, 0, layer);
    B.wait_for(imax(posGX[0], need(posGW, 0, 7))); B.bar();
    for (int sg = 0; sg < GD_NST; ++sg) {
      if (sg + 1 < GD_NST && sg != 13) B.wait_for(imax(need(posGW, gd_begin(sg + 1), gd_end(sg + 1)), sg < 13 ? posGX[(sg + 1) >> 1] : -1));
      B.bar();
      gw_upto(gd_end(sg) + GW_CAP);
      if (sg == 13) { B.wait_for(need(posGW, gd_begin(14), gd_end(14))); B.bar(); }
    }
    B.bar();
    for (int i = 0; i < R_BARS; ++i) { if (layer == 0 && i == FK_SAP_AT) sa_prefetch(); B.bar(); }
  }
  return B.p;
}
constexpr Prog PROG_L = build_prog(0), PROG_X = build_prog(1);
constexpr int count_bars(const Prog& p) { int c = 0; for (int i = 0; i < p.n; ++i) c += p.op[i].kind == OP_BAR; return c; }
static_assert(FK_HW_FROM >= S_TILED_POLL, "H weights are requested only behind the poll of the re-tiling");
static_assert(count_bars(PROG_L) == P0_BARS + 2 * (S_BARS + H_BARS + G_BARS + R_BARS) && count_bars(PROG_X) == count_bars(PROG_L), "helper and compute barrier counts differ");
static_assert(FK_SAP_AT < R_BARS && PROG_L.n < PROG_MAX && PROG_X.n < PROG_MAX, "program");

template <int N> __device__ __forceinline__ void bars(TR_ARG0) { sfor<0, N>([&](auto) { BAR(); }); }

__device__ __forceinline__ void touch1(u32& sink, i32x4 rs, u32 voff, u32 so) { asm volatile("buffer_load_dword %0, %1, %2, %3 offen" : "+v"(sink) : "v"(voff), "s"(rs), "s"(so)); }
__device__ __forceinline__ void keep(u32 v) { asm volatile("" :: "v"(v)); }
template <bool SC1, u32 SIMM> __device__ __forceinline__ void stg_load(u32x4& dst, u32 voff, i32x4 rs, u32 sdyn) {
  u32 tmp;
  if constexpr (SC1) asm volatile("s_add_u32 %1, %4, %5\n\tbuffer_load_dwordx4 %0, %2, %3, %1 offen sc1" : "=v"(dst), "=&s"(tmp) : "v"(voff), "s"(rs), "s"(sdyn), "n"(SIMM) : "scc");
  else asm volatile("s_add_u32 %1, %4, %5\n\tbuffer_load_dwordx4 %0, %2, %3, %1 offen" : "=v"(dst), "=&s"(tmp) : "v"(voff), "s"(rs), "s"(sdyn), "n"(SIMM) : "scc");
}
template <u32 IMM> __device__ __forceinline__ void stg_write(u32 vaddr, const u32x4& src) { asm volatile("ds_write_b128 %0, %1 offset:%2" :: "v"(vaddr), "v"(src), "n"(IMM) : "memory"); }

template <int ROLE> __device__ __forceinline__ void helper_role(const Params& p, const Roles& R, const int l, const int lane TR_ARG) {
  const i32x4 rCr = make_rsrc(ROLE == 0 && l ? p.Ci : p.Cr, SEQ * SEQ * 2);
  const i32x4 rCi = make_rsrc(p.Ci, SEQ * SEQ * 2);
  const i32x4 rW = make_rsrc(p.ws, WS_USED);
  u32 vSA[2];
#pragma unroll
  for (int pr = 0; pr < 2; ++pr)
    vSA[pr] = (u32)((R.s_tt * 64 + (lane >> 3)) * (SEQ * 2) + R.s_kh * 2048 + (((lane & 7) ^ ((4 * pr + (lane >> 4)) & 7)) * 16));
  const u32 l16 = (u32)lane * 16;
  const u32 vHW = l16 + l * 14336 + OFF_WH + R.h_cc * WH_CC;
  const u32 vGW = l16 + l * 2048 + OFF_WFF + R.g_fs * WFF_FS;
  const u32 vSB0 = l16 + l * 7168 + OFF_XN + R.s_ec * (64 * 7168) + R.s_kh * (32 * 7168);
  u32 vGX[2];
#pragma unroll
  for (int pr = 0; pr < 2; ++pr)
    vGX[pr] = OFF_X1R + (u32)((R.g_rb * 64 + 32 * l + (lane >> 3)) * (DM * 2) + (((lane & 7) ^ ((4 * pr + (lane >> 4)) & 7)) * 16));
  const u32 bSA = L_SA + l * 8192, bSB = L_SB + l * 7168, bHW = L_HW + l * 2 * HW_CAP * 1024, bGW = L_GW + l * 2 * GW_CAP * 1024;
  const u32 bGX = L_GX + l * 4096;
  const u32 wSA[2] = {l16 + bSA, l16 + bSA + 65536}, wGW[2] = {l16 + bGW, l16 + bGW + 65536};
  const u32 rot = __builtin_amdgcn_readfirstlane(FK_ROT(R.s_tt));
  const u32 zero = freshs(0);
  u32x4 stg[STG];
  u32 sink = 0;
  const u32 vT = (u32)((R.s_tt * 64 + R.s_ec * 16 + (lane >> 5)) * (SEQ * 2) + R.s_kh * 2048 + (lane & 31) * 64);
  constexpr const Prog& P = ROLE == 0 ? PROG_L : PROG_X;
  sfor<0, P.n>([&](auto ic) {
    constexpr Op o = P.op[ic];
    if constexpr (o.kind == OP_BAR) BAR();
    else if constexpr (o.kind == OP_WAIT) wait_v<o.a>();
    else if constexpr (o.kind == OP_WAITL) WAIT_L0();
    else if constexpr (o.kind == OP_SA) dma16s<false, (o.a % SA_N) * SA_ST + o.b * 1024, o.b * 32768>(bSA, vSA[o.b & 1], rCr, ((freshs(rot) + o.a) & 15) * 128);
    else if constexpr (o.kind == OP_SB) dma16s<true, (o.a % SB_N) * SB_ST + o.b * 1024, o.b * 1024 + o.c * ACT_B>(bSB, vSB0, rW, ((freshs(rot) + o.a) & 15) * 14336);
    else if constexpr (o.kind == OP_HW) dma16<true, (o.b * HW_CAP + hw_slot(o.b, o.a)) * 1024, ((o.a / 7) * 28 + 7 * o.b + o.a % 7) * 1024>(bHW, vHW, rW);
    else if constexpr (o.kind == OP_GX) dma16<true, (o.a / 4) * 8192 + (o.a % 4) * 1024, (o.a % 4) * 8 * (DM * 2) + (o.a / 4) * 128 + o.c * ACT_B>(bGX, vGX[o.a & 1], rW);
    else if constexpr (o.kind == OP_GW) dma16<true, (o.b * GW_CAP + o.a % GW_CAP) * 1024, (o.a * 4 + o.b) * 1024>(bGW, vGW, rW);
    else if constexpr (o.kind == OP_LSA) stg_load<false, o.b * 32768>(stg[o.d], vSA[o.b & 1], o.c > 0 ? rCi : rCr, ((freshs(rot) + o.a) & 15) * 128);
    else if constexpr (o.kind == OP_WSA) { constexpr u32 off = (o.a % SA_N) * SA_ST + (o.c > 0 ? 8192 : 0) + o.b * 1024; stg_write<off % 65536>(wSA[off / 65536], stg[o.d]); }
    else if constexpr (o.kind == OP_LGW) stg_load<true, (o.a * 4 + o.b) * 1024>(stg[o.d], vGW, rW, zero);
    else if constexpr (o.kind == OP_TOUCH) touch1(sink, o.b ? rCi : rCr, vT, freshs(o.a * 2 * SEQ * 2));
    else if constexpr (o.kind == OP_SLEEP) __builtin_amdgcn_s_sleep(8);
    else if constexpr (o.kind == OP_SINK) keep(sink);
    else if constexpr (o.kind == OP_WGW) { constexpr u32 off = (o.b * GW_CAP + o.a % GW_CAP) * 1024; stg_write<off % 65536>(wGW[off / 65536], stg[o.d]); }
  });
}

struct Sync {
  GAS char* ws; u64 salt; bool dead; int lane;
  __device__ __forceinline__ void give_up() {
    if (lane == 0) __hip_atomic_store((GAS u64*)(ws + OFF_ABORT), salt + 63, __ATOMIC_RELAXED, __HIP_MEMORY_SCOPE_AGENT);
    dead = true;
  }
  template <bool TWO> __device__ __forceinline__ void poll(u32 off, int n, u64 tag) {
    if (dead) return;
    const u64 t0 = __builtin_amdgcn_s_memrealtime();
    for (u32 spins = 1;; ++spins) {
      u64 v = tag, v2 = tag;
      if (lane < n) v = __hip_atomic_load((GAS u64*)(ws + off + lane * 8), __ATOMIC_RELAXED, __HIP_MEMORY_SCOPE_AGENT);
      if (TWO) v2 = __hip_atomic_load((GAS u64*)(ws + off + (lane + 64) * 8), __ATOMIC_RELAXED, __HIP_MEMORY_SCOPE_AGENT);
      if (__all(v == tag && v2 == tag)) break;
      if ((spins & 63u) == 0u) {
        const u64 ab = __hip_atomic_load((GAS u64*)(ws + OFF_ABORT), __ATOMIC_RELAXED, __HIP_MEMORY_SCOPE_AGENT);
        if (__all(ab == salt + 63)) { dead = true; return; }
        if (__builtin_amdgcn_s_memrealtime() - t0 > 400000ull) { give_up(); return; }
      }
    }
    __builtin_amdgcn_fence(__ATOMIC_ACQUIRE, "wavefront");
  }
  __device__ __forceinline__ void post(u32 off, u64 tag) {
    if (lane == 0) __hip_atomic_store((GAS u64*)(ws + off), tag, __ATOMIC_RELAXED, __HIP_MEMORY_SCOPE_AGENT);
  }
};

#define STAMP(k) do { } while (0)
__device__ __forceinline__ void sync_role(const Params& p, const Roles& R, const bool active, const int lane TR_ARG) {
  Sync sy; sy.ws = (GAS char*)p.ws; sy.lane = lane; sy.dead = !active; sy.salt = 0;
  STAMP(0);
  if (active) sy.salt = __hip_atomic_load((GAS u64*)(sy.ws + OFF_SALT), __ATOMIC_RELAXED, __HIP_MEMORY_SCOPE_AGENT);
  const u64 salt = sy.salt;
  bars<2>(TR_PASS0); if (active) sy.post(OFF_FP0A + R.b * 8, salt + 1);
  STAMP(1);
  bars<P0_BARS - 2>(TR_PASS0);
  STAMP(2);
#pragma unroll 1
  for (int layer = 0; layer < 2; ++layer) {
    const int sb = 3 + layer * 16; (void)sb;
    sy.template poll<true>((layer ? OFF_FR : OFF_FP0A) + R.s_kh * 1024, 64, layer ? salt + 3 : salt + 1);
    STAMP(sb + 0);
    bars<2>(TR_PASS0);
    STAMP(sb + 1);
    bars<S_TILED_AT + 1>(TR_PASS0);
    if (active && layer == 0) sy.post(OFF_FP0B + R.b * 8, salt + 2);
    bars<S_TILED_POLL - S_TILED_AT - 1>(TR_PASS0);
    if (layer == 0) {
      sy.template poll<true>(OFF_FP0B, 64, salt + 2);
      sy.template poll<true>(OFF_FP0B + 1024, 64, salt + 2);
      if (R.b == 0 && !sy.dead) sy.post(OFF_SALT, salt + 64);
    }
    bars<S_BARS - 3 - S_TILED_POLL>(TR_PASS0);
    STAMP(sb + 2);
    bars<1>(TR_PASS0);
    if (active) sy.post(OFF_FS + (layer * 32 + R.s_tt) * 128 + (R.s_kh * 4 + R.s_ec) * 8, salt + 4 + layer);
    STAMP(sb + 3);
    sy.template poll<false>(OFF_FS + (layer * 32 + (R.h_rt >> 1)) * 128, 8, salt + 4 + layer);
    STAMP(sb + 4);
    bars<2>(TR_PASS0);
    STAMP(sb + 5);
    bars<7>(TR_PASS0);
    STAMP(sb + 6);
    bars<H_BARS - 9>(TR_PASS0);
    if (active) sy.post(OFF_FH + (layer * 32 + (R.h_rt >> 1)) * 128 + ((R.h_rt & 1) * 4 + R.h_cc) * 8, salt + 6 + layer);
    STAMP(sb + 7);
    sy.template poll<false>(OFF_FH + (layer * 32 + R.g_rb) * 128, 8, salt + 6 + layer);
    STAMP(sb + 8);
    bars<2>(TR_PASS0);
    STAMP(sb + 9);
    bars<14>(TR_PASS0);
    STAMP(sb + 10);
    bars<1>(TR_PASS0);
    STAMP(sb + 11);
    bars<8>(TR_PASS0);
    STAMP(sb + 12);
    bars<G_BARS - 25>(TR_PASS0);
    if (active) sy.post(OFF_FG + (layer * 32 + R.g_rb) * 128 + R.g_fs * 8, salt + 8 + layer);
    STAMP(sb + 13);
    sy.template poll<false>(OFF_FG + (layer * 32 + (R.b >> 3)) * 128, 8, salt + 8 + layer);
    STAMP(sb + 14);
    bars<R_BARS>(TR_PASS0);
    if (active && layer == 0) sy.post(OFF_FR + R.b * 8, salt + 3);
    STAMP(sb + 15);
  }
}

#define FSTAMP(k) do { } while (0)
#define FSTAMP_(ph, k) do { } while (0)
#define FINE_WS(p) do { } while (0)
__device__ __forceinline__ void ln_row_to_lds(LAS char* L, const float (&x)[8], int w, int lane) {
  const bool on = lane < 56;
  float s = 0.f;
#pragma unroll
  for (int j = 0; j < 8; ++j) s += x[j];
  const float mean = sum64(on ? s : 0.f) * (1.f / DM);
  float q = 0.f;
#pragma unroll
  for (int j = 0; j < 8; ++j) { const float d = x[j] - mean; q += d * d; }
  const float rstd = __builtin_amdgcn_rsqf(sum64(on ? q : 0.f) * (1.f / DM) + LN_EPS);
  u32x4 o;
#pragma unroll
  for (int j = 0; j < 4; ++j) o[j] = pk_bf16((x[2 * j] - mean) * rstd, (x[2 * j + 1] - mean) * rstd);
  if (on) lds_st<u32x4>(L, L_T + w * T_PITCH + lane * 16, o);
}
__device__ __forceinline__ void xn_store(LAS char* L, rsrc_t rW, u32 base, int b, int e) {
  if (e < DM) {
    u32 h[8];
#pragma unroll
    for (int j = 0; j < 8; ++j) h[j] = lds_ld<unsigned short>(L, L_T + j * T_PITCH + e * 2);
    const int ec = e / 112, T = (e % 112) >> 4, el = e & 15;
    st16_sc1(rW, base + (u32)(((ec * 64 + (b >> 2)) * 7 + T) * 1024 + ((b & 3) * 16 + el) * 16),
             u32x4{h[0] | (h[1] << 16), h[2] | (h[3] << 16), h[4] | (h[5] << 16), h[6] | (h[7] << 16)});
  }
}

__device__ __forceinline__ void p0_comp(const Params& p, const Roles& R, LAS char* L, rsrc_t rW, int w, int lane TR_ARG) {
  const int tid = w * 64 + lane, b = R.b;
  u32x4 xr = {0, 0, 0, 0};
  if (lane < 56) xr = *(const GAS u32x4*)((const GAS char*)p.x0 + (size_t)(8 * b + w) * (DM * 2) + lane * 16);
  float x[8];
#pragma unroll
  for (int j = 0; j < 4; ++j) { x[2 * j] = bf_lo(xr[j]); x[2 * j + 1] = bf_hi(xr[j]); }
  ln_row_to_lds(L, x, w, lane);
  WAIT_L0(); BAR();
  xn_store(L, rW, OFF_XN, b, tid);
  WAIT_V0(); BAR();
  const int kind = b < 112 ? 0 : b < 168 ? 1 : b < 196 ? 2 : 3;
  u32x4 it[4];
  if (kind == 0) {
    const int fs = b / 14, js = (b % 14) >> 1, hf = b & 1, m = (tid >> 2) & 1, ch = tid & 3;
#pragma unroll
    for (int i = 0; i < 4; ++i) it[i] = *(const GAS u32x4*)((const GAS char*)(m ? p.Wu : p.Wg) + (size_t)(224 * hf + imin((tid >> 3) + 64 * i, 223)) * (FF * 2) + fs * 448 + js * 64 + ch * 16);
  } else if (kind == 1) {
    const int fs = (b - 112) / 7, k = (b - 112) % 7;
#pragma unroll
    for (int i = 0; i < 4; ++i) { const int c = imin(tid + 512 * i, 1791), r = c / 56, ch = c % 56, q = r >> 3, j = r & 7;
      const int f = 224 * fs + 8 * (7 * (j >> 1) + k) + 2 * q + (j & 1);
      it[i] = *(const GAS u32x4*)((const GAS char*)p.Wd + (size_t)f * (DM * 2) + ch * 16); }
  } else if (kind == 2) {
    const int cc = (b - 168) / 7, tp = (b - 168) % 7;
#pragma unroll
    for (int i = 0; i < 4; ++i) { const int c = imin(tid + 512 * i, 1791), m = c / 896, r = (c % 896) / 56, ch = c % 56;
      it[i] = *(const GAS u32x4*)((const GAS char*)(m ? p.Wi : p.Wr) + (size_t)(112 * cc + 16 * tp + r) * (DM * 2) + ch * 16); }
  }
  if (kind == 0) {
    const int m = (tid >> 2) & 1, ch = tid & 3;
    float s[8] = {0, 0, 0, 0, 0, 0, 0, 0};
#pragma unroll
    for (int i = 0; i < 4; ++i) {
      const int row = (tid >> 3) + 64 * i;
      if (row < 224) {
        lds_st<u32x4>(L, L_SCR + m * 14400 + row * 64 + (ch ^ ((row >> 3) & 3)) * 16, it[i]);
#pragma unroll
        for (int j = 0; j < 4; ++j) { s[2 * j] += bf_lo(it[i][j]); s[2 * j + 1] += bf_hi(it[i][j]); }
      }
    }
#pragma unroll
    for (int j = 0; j < 8; ++j) {
      float v = dpp_add<0x128>(s[j]);
      const auto a = __builtin_amdgcn_permlane16_swap(__builtin_bit_cast(u32, v), __builtin_bit_cast(u32, v), false, false);
      v = as_f(a[0]) + as_f(a[1]);
      const auto c = __builtin_amdgcn_permlane32_swap(__builtin_bit_cast(u32, v), __builtin_bit_cast(u32, v), false, false);
      s[j] = as_f(c[0]) + as_f(c[1]);
    }
    if (lane < 8) { lds_st<f32x4>(L, L_MISC + (w * 8 + lane) * 32, f32x4{s[0], s[1], s[2], s[3]}); lds_st<f32x4>(L, L_MISC + (w * 8 + lane) * 32 + 16, f32x4{s[4], s[5], s[6], s[7]}); }
  } else if (kind == 1) {
#pragma unroll
    for (int i = 0; i < 4; ++i) { const int c = tid + 512 * i, r = c / 56, ch = c % 56; if (c < 1792) lds_st<u32x4>(L, L_SCR + r * 912 + ch * 16, it[i]); }
  } else if (kind == 2) {
#pragma unroll
    for (int i = 0; i < 4; ++i) { const int c = tid + 512 * i, m = c / 896, r = (c % 896) / 56, ch = c % 56; if (c < 1792) lds_st<u32x4>(L, L_SCR + (m * 16 + r) * 912 + ch * 16, it[i]); }
  }
  WAIT_L0(); BAR();
  if (kind == 0) {
    const int fs = b / 14, js = (b % 14) >> 1, hf = b & 1;
#pragma unroll
    for (int i = 0; i < 4; ++i) { const int c = tid + 512 * i, fr = c >> 6, ln = c & 63, m = ln & 15, q = ln >> 4, ks = fr >> 2, tt = fr & 3;
      if (c < 1792) {
        u32 h[8];
#pragma unroll
        for (int j = 0; j < 8; ++j) h[j] = lds_ld<unsigned short>(L, L_SCR + ((m >> 1) & 1) * 14400 + (32 * ks + 8 * q + j) * 64 + (tt ^ q) * 16 + (2 * (m >> 2) + (m & 1)) * 2);
        const int tau = 4 * js + tt, wv = tau / 7, n = (7 * hf + ks) * 7 + tau % 7;
        st16_sc1(rW, OFF_WFF + fs * WFF_FS + (u32)(n * 4 + wv) * 1024 + ln * 16, u32x4{h[0] | (h[1] << 16), h[2] | (h[3] << 16), h[4] | (h[5] << 16), h[6] | (h[7] << 16)}); } }
    if (tid < 64) {
      const int m = tid >> 5, c32 = tid & 31;
      float s = 0.f;
#pragma unroll
      for (int wv = 0; wv < 8; ++wv) s += lds_ld<float>(L, L_MISC + (wv * 8 + m * 4 + (c32 >> 3)) * 32 + (c32 & 7) * 4);
      const int f = js * 32 + c32;
      __hip_atomic_store((GAS u32*)(p.ws + OFF_CS + hf * 16384 + (u32)((fs * 28 + f / 8) * 16 + ((f & 7) >> 1) * 4 + m * 2 + (f & 1)) * 4), __builtin_bit_cast(u32, s), __ATOMIC_RELAXED, __HIP_MEMORY_SCOPE_AGENT);
    }
  } else if (kind == 1) {
    const int fs = (b - 112) / 7, k = (b - 112) % 7;
#pragma unroll
    for (int i = 0; i < 4; ++i) { const int c = tid + 512 * i, tau = c >> 6, ln = c & 63, m = ln & 15, q = ln >> 4, wv = tau / 7, tp = tau % 7;
      if (c < 1792) {
        const int col = 112 * wv + (tp < 6 ? 32 * (tp >> 1) + 8 * (m >> 2) + 4 * (tp & 1) + (m & 3) : 96 + m);
        u32 h[8];
#pragma unroll
        for (int j = 0; j < 8; ++j) h[j] = lds_ld<unsigned short>(L, L_SCR + (8 * q + j) * 912 + col * 2);
        const int n = tp < 4 ? 98 + k * 4 + tp : 126 + k * 3 + tp - 4;
        st16_sc1(rW, OFF_WFF + fs * WFF_FS + (u32)(n * 4 + wv) * 1024 + ln * 16, u32x4{h[0] | (h[1] << 16), h[2] | (h[3] << 16), h[4] | (h[5] << 16), h[6] | (h[7] << 16)}); } }
  } else if (kind == 2) {
    const int cc = (b - 168) / 7, tp = (b - 168) % 7;
#pragma unroll
    for (int i = 0; i < 4; ++i) { const int c = tid + 512 * i, fr = c >> 6, ln = c & 63, m = ln & 15, q = ln >> 4, T = fr >> 2, ec = fr & 3;
      if (c < 1792) {
        const u32 o = (u32)(m * 912 + (112 * ec + 16 * T + 4 * q) * 2);
        const u32x2 re = lds_ld<u32x2>(L, L_SCR + o), im = lds_ld<u32x2>(L, L_SCR + 16 * 912 + o);
        st16_sc1(rW, OFF_WH + cc * WH_CC + (u32)((T * 4 + ec) * 7 + tp) * 1024 + ln * 16, u32x4{re.x, re.y, im.x ^ 0x80008000u, im.y ^ 0x80008000u}); } }
  }
}

template <int NT> __device__ __forceinline__ void s_body(const Params& p, const Roles& R, LAS char* L, rsrc_t rW, int layer, int w, int rg, int t0, int lane TR_ARG) {
  FINE_WS(p.ws);
  const int fr = lane & 15, fq = lane >> 4, rr = rg * 16 + fr;
  const u32 a0 = freshu(L_SA + (u32)(rr * 128 + ((fq ^ ((rr >> 1) & 7)) * 16))), a1 = freshu(L_SA + (u32)(rr * 128 + (((4 + fq) ^ ((rr >> 1) & 7)) * 16)));
  const u32 b0 = freshu(L_SB + (u32)(t0 * 1024 + lane * 16));
  f32x4 accP[NT], accQ[NT];
#pragma unroll
  for (int t = 0; t < NT; ++t) { accP[t] = f32x4{0, 0, 0, 0}; accQ[t] = f32x4{0, 0, 0, 0}; }
  bf16x8 ap[2][2], aq[2][2], bb[2][2][NT];
  auto rd = [&](int s, int n) {
    const u32 sa = (u32)(s % SA_N) * SA_ST, sb = (u32)(s % SB_N) * SB_ST;
    ap[n][0] = lds_ld<bf16x8>(L, a0 + sa); aq[n][0] = lds_ld<bf16x8>(L, a0 + sa + 8192);
#pragma unroll
    for (int t = 0; t < NT; ++t) bb[n][0][t] = lds_ld<bf16x8>(L, b0 + sb + t * 1024);
    ap[n][1] = lds_ld<bf16x8>(L, a1 + sa); aq[n][1] = lds_ld<bf16x8>(L, a1 + sa + 8192);
#pragma unroll
    for (int t = 0; t < NT; ++t) bb[n][1][t] = lds_ld<bf16x8>(L, b0 + sb + (7 + t) * 1024);
  };
  BAR(); BAR();
  rd(0, 0);
  sfor<0, 16>([&](auto sc) { constexpr int s = sc, c = s & 1;
    if constexpr (s == S_TILED_AT) WAIT_V0();
    WAIT_L0(); BAR();
    if constexpr (s == 15) FSTAMP_(1, 0);
    if constexpr (s < 15) rd(s + 1, c ^ 1);
#pragma unroll
    for (int k = 0; k < 2; ++k)
#pragma unroll
      for (int t = 0; t < NT; ++t) { accP[t] = mfma(bb[c][k][t], ap[c][k], accP[t]); accQ[t] = mfma(bb[c][k][t], aq[c][k], accQ[t]); }
    if constexpr (s < 15) {
#pragma unroll
      for (int i = 0; i < 4 + 2 * NT; ++i) { __builtin_amdgcn_sched_group_barrier(0x8, 1, 0); __builtin_amdgcn_sched_group_barrier(0x100, 1, 0); }
      __builtin_amdgcn_sched_group_barrier(0x8, 2 * NT - 4, 0);
    }
    SCHED_FENCE();
  });
  asm volatile("" : "+v"(accP[0]), "+v"(accQ[NT - 1]));
  FSTAMP_(1, 1);
  const u32 o = OFF_PQ + layer * PQ_L + (u32)((R.s_kh * 128 + R.s_tt * 4 + rg) * 4 + R.s_ec) * 7168 + t0 * 1024 + lane * 16;
#pragma unroll
  for (int t = 0; t < NT; ++t)
    st16_sc1(rW, o + t * 1024, u32x4{pk_bf16(accP[t][0], accP[t][1]), pk_bf16(accP[t][2], accP[t][3]), pk_bf16(accQ[t][0], accQ[t][1]), pk_bf16(accQ[t][2], accQ[t][3])});
  FSTAMP_(1, 2);
  WAIT_V0();
  FSTAMP_(1, 3);
  BAR();
  FSTAMP_(1, 4);
}
__device__ __forceinline__ void s_comp(const Params& p, const Roles& R, LAS char* L, rsrc_t rW, int layer, int w, int lane TR_ARG) {
  if (w < 4) s_body<4>(p, R, L, rW, layer, w, w, 0, lane TR_PASS);
  else s_body<3>(p, R, L, rW, layer, w, w - 4, 4, lane TR_PASS);
}

template <int PAR> __device__ __forceinline__ void h_body(const Params& p, const Roles& R, LAS char* L, rsrc_t rW, int layer, int w, int lane TR_ARG) {
  const int tid = w * 64 + lane, ec = w & 3, i16 = w >> 2;
  FINE_WS(p.ws);
  f32x4 acc[7];
#pragma unroll
  for (int t = 0; t < 7; ++t) acc[t] = f32x4{0, 0, 0, 0};
  bf16x8 ww[2][7];
  const u32 l16 = (u32)lane * 16;
  const u32 wb = freshu(L_HW + (u32)ec * (HW_CAP * 1024) + l16);
  auto rdw = [&](int T, int n) {
#pragma unroll
    for (int t = 0; t < 7; ++t) ww[n][t] = lds_ld<bf16x8>(L, wb + hw_slot(PAR, 7 * T + t) * 1024);
  };
  BAR();
  constexpr int H_AHEAD = 3;
  u32x4 aa[7][2];
  const u32 ao = OFF_PQ + layer * PQ_L + (u32)(((2 * R.h_rt + i16) * 4 + ec) * 7) * 1024 + l16;
  auto lda = [&](int T) {
#pragma unroll
    for (int kh = 0; kh < 2; ++kh) aa[T][kh] = ld16_sc1(rW, ao + (u32)(kh * 128 * 28 + T) * 1024);
  };
#pragma unroll
  for (int T = 0; T < H_AHEAD; ++T) lda(T);
  const int g = tid & 15, rl = tid >> 4;
  const u32 ro = (u32)((32 * R.h_rt + rl) * DM + 112 * R.h_cc + 8 * (g < 14 ? g : 13)) * 2;
  u32x4 rs;
  if (layer == 0) rs = *(const GAS u32x4*)((const GAS char*)p.x0 + ro);
  else rs = ld16_sc1(rW, OFF_X2 + ro);
  SCHED_FENCE();
  BAR();
  rdw(0, 0);
  sfor<0, 7>([&](auto tc) { constexpr int T = tc, c = T & 1;
    WAIT_L0(); BAR();
    if constexpr (T + H_AHEAD < 7) lda(T + H_AHEAD);
    if constexpr (T < 6) rdw(T + 1, c ^ 1);
#pragma unroll
    for (int kh = 0; kh < 2; ++kh)
#pragma unroll
      for (int t = 0; t < 7; ++t) acc[t] = mfma(ww[c][t], __builtin_bit_cast(bf16x8, aa[T][kh]), acc[t]);
    if constexpr (T < 6) {
      if constexpr (T + H_AHEAD < 7) __builtin_amdgcn_sched_group_barrier(0x20, 2, 0);
#pragma unroll
      for (int i = 0; i < 7; ++i) { __builtin_amdgcn_sched_group_barrier(0x8, 1, 0); __builtin_amdgcn_sched_group_barrier(0x100, 1, 0); }
      __builtin_amdgcn_sched_group_barrier(0x8, 7, 0);
    }
    SCHED_FENCE();
  });
  FSTAMP(0);
#pragma unroll
  for (int t = 0; t < 7; ++t) {
    const int row = lane & 15, q = lane >> 4, gs = 2 * t + (q >> 1);
    lds_st<f32x4>(L, L_RED + (u32)((w * 7 + t) * 1024 + (q * 16 + (row & 1) * 8 + (((row >> 1) + gs) & 7)) * 16), acc[t]);
  }
  WAIT_L0();
  FSTAMP(1);
  BAR();
  FSTAMP(2);
  {
    const int gg = g < 14 ? g : 13, tp = gg >> 1, q0 = 2 * (gg & 1);
    f32x4 v0 = {0, 0, 0, 0}, v1 = {0, 0, 0, 0};
#pragma unroll
    for (int e = 0; e < 4; ++e) {
      const u32 o = L_RED + (u32)((((rl >> 4) * 4 + e) * 7 + tp) * 1024 + (q0 * 16 + (rl & 1) * 8 + ((((rl & 15) >> 1) + gg) & 7)) * 16);
      v0 += lds_ld<f32x4>(L, o); v1 += lds_ld<f32x4>(L, o + 256);
    }
    asm volatile("" : "+v"(v0), "+v"(v1));
    FSTAMP(3);
    u32x4 o4;
    o4.x = pk_bf16(bf_lo(rs.x) + v0[0], bf_hi(rs.x) + v0[1]); o4.y = pk_bf16(bf_lo(rs.y) + v0[2], bf_hi(rs.y) + v0[3]);
    o4.z = pk_bf16(bf_lo(rs.z) + v1[0], bf_hi(rs.z) + v1[1]); o4.w = pk_bf16(bf_lo(rs.w) + v1[2], bf_hi(rs.w) + v1[3]);
    const bool on = g < 14;
    const int row = 32 * R.h_rt + rl;
    if (on) st16_sc1(rW, OFF_X1R + layer * ACT_B + ro, o4);
    float x[8];
#pragma unroll
    for (int j = 0; j < 4; ++j) { x[2 * j] = bf_lo(o4[j]); x[2 * j + 1] = bf_hi(o4[j]); }
    float s = 0.f;
#pragma unroll
    for (int j = 0; j < 8; ++j) s += x[j];
    const float mc = sum16(on ? s : 0.f) * (1.f / 112.f);
    float q = 0.f;
#pragma unroll
    for (int j = 0; j < 8; ++j) { const float d = x[j] - mc; q += d * d; }
    const float m2 = sum16(on ? q : 0.f);
    if (g == 0) st8_sc1(rW, OFF_STATS + layer * STATS_L + (u32)(row * 4 + R.h_cc) * 8, u32x2{__builtin_bit_cast(u32, mc), __builtin_bit_cast(u32, m2)});
  }
  FSTAMP(4);
  WAIT_V0();
  FSTAMP(5);
  BAR();
  FSTAMP(6);
}

__device__ __forceinline__ void h_comp(const Params& p, const Roles& R, LAS char* L, rsrc_t rW, int layer, int w, int lane TR_ARG) {
  if (w & 1) h_body<1>(p, R, L, rW, layer, w, lane TR_PASS);
  else h_body<0>(p, R, L, rW, layer, w, lane TR_PASS);
}

__device__ __forceinline__ void gd_comp(const Params& p, const Roles& R, LAS char* L, rsrc_t rW, int layer, int w, int lane TR_ARG) {
  FINE_WS(p.ws);
  const int fr = lane & 15, fq = lane >> 4, c4 = w & 3, r = w >> 2;
  const u32 l16 = (u32)lane * 16;
  f32x4 acc[2][7];
  bf16x8 xx[2][4], ww[2][8];
  const u32 wb = freshu(L_GW + (u32)c4 * (GW_CAP * 1024) + l16);
  auto rdw = [&](auto sc, int n) {
    constexpr int sg = sc;
#pragma unroll
    for (int t = 0; t < gd_end(sg) - gd_begin(sg); ++t) ww[n][t] = lds_ld<bf16x8>(L, wb + ((gd_begin(sg) + t) % GW_CAP) * 1024);
  };
  const u32 xb0 = freshu(L_GX + (u32)((32 * r + fr) * 128 + ((fq ^ ((fr >> 1) & 7)) * 16))), xb1 = freshu(L_GX + (u32)((32 * r + fr) * 128 + (((4 + fq) ^ ((fr >> 1) & 7)) * 16)));
  auto rdx = [&](int ks, int n) {
#pragma unroll
    for (int i = 0; i < 2; ++i) xx[n][i] = lds_ld<bf16x8>(L, ((ks & 1) ? xb1 : xb0) + (ks >> 1) * 8192 + i * 2048);
  };
  const u32 hr = freshu(L_GX + (u32)r * 2048 + l16), hw = freshu(L_GX + (u32)r * 2048 + l16 + c4 * 4);
  auto rdh = [&](int k, int n, int half) {
#pragma unroll
    for (int i = 0; i < 2; ++i) xx[n][half * 2 + i] = lds_ld<bf16x8>(L, hr + (u32)(k * 4 + i) * 1024);
  };
  BAR();
  FSTAMP_(2, 0);
  float var[2], c1[2];
  {
    u32x4 st[2][2];
#pragma unroll
    for (int i = 0; i < 2; ++i) {
      const u32 o = OFF_STATS + layer * STATS_L + (u32)(64 * R.g_rb + 32 * r + 16 * i + fr) * 32;
      st[i][0] = ld16_sc1(rW, o); st[i][1] = ld16_sc1(rW, o + 16);
    }
    f32x4 cs[7];
#pragma unroll
    for (int t = 0; t < 7; ++t) { const u32 o = OFF_CS + (u32)((R.g_fs * 28 + c4 * 7 + t) * 16 + 4 * fq) * 4; cs[t] = __builtin_bit_cast(f32x4, ld16_sc1(rW, o)) + __builtin_bit_cast(f32x4, ld16_sc1(rW, o + 16384)); }
#pragma unroll
    for (int i = 0; i < 2; ++i) {
      const float m0 = as_f(st[i][0].x), m1 = as_f(st[i][0].z), m2 = as_f(st[i][1].x), m3 = as_f(st[i][1].z);
      const float mean = 0.25f * ((m0 + m1) + (m2 + m3));
      const float d0 = m0 - mean, d1 = m1 - mean, d2 = m2 - mean, d3 = m3 - mean;
      const float M2 = (as_f(st[i][0].y) + as_f(st[i][0].w)) + (as_f(st[i][1].y) + as_f(st[i][1].w)) + 112.f * ((d0 * d0 + d1 * d1) + (d2 * d2 + d3 * d3));
      var[i] = M2 * (1.f / DM) + LN_EPS;
      c1[i] = -1.4426950408889634f * __builtin_amdgcn_rsqf(var[i]);
#pragma unroll
      for (int t = 0; t < 7; ++t) acc[i][t] = cs[t] * (-mean);
    }
  }
  SCHED_FENCE();
  FSTAMP_(2, 1);
  BAR();
  FSTAMP_(2, 2);
  rdx(0, 0);
  rdw(std::integral_constant<int, 0>{}, 0);
  sfor<0, 14>([&](auto jc) { constexpr int j = jc, c = j & 1;
    WAIT_L0(); BAR();
    if constexpr (j < 13) { rdx(j + 1, c ^ 1); rdw(std::integral_constant<int, j + 1>{}, c ^ 1); }
#pragma unroll
    for (int t = 0; t < 7; ++t)
#pragma unroll
      for (int i = 0; i < 2; ++i) acc[i][t] = mfma(ww[c][t], xx[c][i], acc[i][t]);
    if constexpr (j < 13) {
#pragma unroll
      for (int i = 0; i < 9; ++i) { __builtin_amdgcn_sched_group_barrier(0x8, 1, 0); __builtin_amdgcn_sched_group_barrier(0x100, 1, 0); }
      __builtin_amdgcn_sched_group_barrier(0x8, 5, 0);
    }
    SCHED_FENCE();
  });
  asm volatile("" : "+v"(acc[0][0]), "+v"(acc[1][6]));
  FSTAMP_(2, 3);
#pragma unroll
  for (int t = 0; t < 7; ++t)
#pragma unroll
    for (int i = 0; i < 2; ++i) {
      const f32x4 a = acc[i][t];
      const f32x2 gg = {a[0], a[1]}, uu = {a[2], a[3]};
      const f32x2 z = gg * c1[i];
      const f32x2 e = {__builtin_amdgcn_exp2f(z[0]), __builtin_amdgcn_exp2f(z[1])};
      const f32x2 dn = e * var[i] + var[i];
      const f32x2 rc = {__builtin_amdgcn_rcpf(dn[0]), __builtin_amdgcn_rcpf(dn[1])};
      const f32x2 h = (gg * uu) * rc;
      lds_st<u32>(L, hw + (u32)(t * 4 + i) * 1024, pk_bf16(h[0], h[1]));
    }
  WAIT_L0();
  FSTAMP_(2, 4);
  BAR();
  FSTAMP_(2, 5);
  const u32 po = OFF_PART + layer * PART_L + (u32)R.g_fs * PART_FS + (u32)((4 * R.g_rb + 2 * r) * 4 + c4) * 4096;
  auto put = [&](const f32x4 lo, const f32x4 hi, int i, int pp) {
    st16_sc1(rW, po + (u32)i * 16384 + pp * 1024 + l16, u32x4{pk_bf16(lo[0], lo[1]), pk_bf16(lo[2], lo[3]), pk_bf16(hi[0], hi[1]), pk_bf16(hi[2], hi[3])});
  };
  f32x4 aA[2][4], aB[2][3];
#pragma unroll
  for (int t = 0; t < 8; ++t) aA[t >> 2][t & 3] = f32x4{0, 0, 0, 0};
#pragma unroll
  for (int t = 0; t < 6; ++t) aB[t / 3][t % 3] = f32x4{0, 0, 0, 0};
  rdh(0, 0, 0); rdh(1, 0, 1);
  rdw(std::integral_constant<int, 14>{}, 0);
  sfor<14, GD_NST>([&](auto sc) {
    constexpr int sg = sc, c = sg & 1;
    constexpr bool second = sg >= 18;
    constexpr int j = second ? sg - 18 : sg - 14, NT = second ? 3 : 4;
    constexpr bool two = j < 3;
    WAIT_L0(); BAR();
    if constexpr (sg + 1 < GD_NST) {
      constexpr int nj = (sg + 1 >= 18) ? sg + 1 - 18 : sg + 1 - 14;
      rdh(2 * nj, c ^ 1, 0);
      if constexpr (nj < 3) rdh(2 * nj + 1, c ^ 1, 1);
      rdw(std::integral_constant<int, sg + 1>{}, c ^ 1);
    }
#pragma unroll
    for (int half = 0; half < (two ? 2 : 1); ++half)
#pragma unroll
      for (int t = 0; t < NT; ++t)
#pragma unroll
        for (int i = 0; i < 2; ++i) {
          if constexpr (second) aB[i][t] = mfma(ww[c][half * NT + t], xx[c][half * 2 + i], aB[i][t]);
          else aA[i][t] = mfma(ww[c][half * NT + t], xx[c][half * 2 + i], aA[i][t]);
        }
    if constexpr (second && j < 2) { put(aA[j][0], aA[j][1], j, 0); put(aA[j][2], aA[j][3], j, 1); }
    {
      constexpr int nm = (two ? 2 : 1) * NT * 2;
      constexpr int nrd = sg + 1 < GD_NST ? gd_end(sg + 1) - gd_begin(sg + 1) + (((sg + 1 >= 18) ? sg + 1 - 18 : sg + 1 - 14) < 3 ? 4 : 2) : 0;
#pragma unroll
      for (int i = 0; i < nm; ++i) {
        __builtin_amdgcn_sched_group_barrier(0x8, 1, 0);
        if (i < nrd) __builtin_amdgcn_sched_group_barrier(0x100, 1, 0);
        if (second && j < 2) __builtin_amdgcn_sched_group_barrier(0x2 | 0x40, 1, 0);
      }
    }
    SCHED_FENCE();
  });
  asm volatile("" : "+v"(aB[0][0]), "+v"(aB[1][2]));
  FSTAMP_(2, 6);
#pragma unroll
  for (int i = 0; i < 2; ++i) {
    put(aB[i][0], aB[i][1], i, 2);
    const u32 d0 = pk_bf16(aB[i][2][0], aB[i][2][1]), d1 = pk_bf16(aB[i][2][2], aB[i][2][3]);
    const auto s0 = __builtin_amdgcn_permlane16_swap(d0, d0, false, false), s1 = __builtin_amdgcn_permlane16_swap(d1, d1, false, false);
    if ((fq & 1) == 0) st16_sc1(rW, po + (u32)i * 16384 + 3072 + (u32)((fq >> 1) * 16 + fr) * 16, u32x4{s0[0], s1[0], s0[1], s1[1]});
  }
  FSTAMP_(2, 7);
  WAIT_V0();
  FSTAMP_(2, 8);
  BAR();
  FSTAMP_(2, 9);
}

__device__ __forceinline__ void r_comp(const Params& p, const Roles& R, LAS char* L, rsrc_t rW, int layer, int w, int lane TR_ARG) {
  const int b = R.b, tid = w * 64 + lane, r8 = lane & 7, sl = lane >> 3, c4 = w & 3, pc = w >> 2;
  const bool on = pc == 0 || sl < 6;
  const int s1 = on ? sl : 5, blk = 2 * pc + (s1 >> 2), qq = s1 & 3, col = 112 * c4 + 32 * blk + 8 * qq;
  const float ncol = pc ? 48.f : 64.f;
  BAR();
  u32x4 v[9];
  const u32 po = OFF_PART + layer * PART_L + (u32)(((b >> 1) * 4 + c4) * 4 + blk) * 1024 + (u32)(qq * 16 + (b & 1) * 8 + r8) * 16;
#pragma unroll
  for (int f = 0; f < 8; ++f) v[f] = ld16_sc1(rW, po + f * PART_FS);
  const u32 ro = (u32)((8 * b + r8) * DM + col) * 2;
  v[8] = ld16_sc1(rW, OFF_X1R + layer * ACT_B + ro);
  float y[8] = {0, 0, 0, 0, 0, 0, 0, 0};
#pragma unroll
  for (int f = 0; f < 8; ++f)
#pragma unroll
    for (int j = 0; j < 4; ++j) { y[2 * j] += bf_lo(v[f][j]); y[2 * j + 1] += bf_hi(v[f][j]); }
  u32x4 o;
#pragma unroll
  for (int j = 0; j < 4; ++j) o[j] = pk_bf16(bf_lo(v[8][j]) + y[2 * j], bf_hi(v[8][j]) + y[2 * j + 1]);
  if (on) {
    if (layer == 0) st16_sc1(rW, OFF_X2 + ro, o);
    else *(GAS u32x4*)((GAS char*)p.out + ro) = o;
  }
  float x[8];
#pragma unroll
  for (int j = 0; j < 4; ++j) { x[2 * j] = bf_lo(o[j]); x[2 * j + 1] = bf_hi(o[j]); }
  auto rowsum = [&](float s) {
    s = dpp_add<0x128>(s);
    const auto a = __builtin_amdgcn_permlane16_swap(__builtin_bit_cast(u32, s), __builtin_bit_cast(u32, s), false, false);
    s = as_f(a[0]) + as_f(a[1]);
    const auto c = __builtin_amdgcn_permlane32_swap(__builtin_bit_cast(u32, s), __builtin_bit_cast(u32, s), false, false);
    return as_f(c[0]) + as_f(c[1]);
  };
  if (layer == 0) {
    float s = 0.f;
#pragma unroll
    for (int j = 0; j < 8; ++j) s += x[j];
    const float mw = rowsum(on ? s : 0.f) / ncol;
    float q = 0.f;
#pragma unroll
    for (int j = 0; j < 8; ++j) { const float d = x[j] - mw; q += d * d; }
    const float m2 = rowsum(on ? q : 0.f);
    if (sl == 0) lds_st<f32x2>(L, L_MISC + (u32)(r8 * 8 + w) * 8, f32x2{mw, m2});
  }
  WAIT_L0(); BAR();
  if (layer == 0) {
    f32x4 s[4];
#pragma unroll
    for (int i = 0; i < 4; ++i) s[i] = lds_ld<f32x4>(L, L_MISC + r8 * 64 + i * 16);
    const float mean = (64.f * ((s[0][0] + s[0][2]) + (s[1][0] + s[1][2])) + 48.f * ((s[2][0] + s[2][2]) + (s[3][0] + s[3][2]))) * (1.f / DM);
    float M2 = 0.f;
#pragma unroll
    for (int i = 0; i < 4; ++i) { const float n = i < 2 ? 64.f : 48.f, d0 = s[i][0] - mean, d1 = s[i][2] - mean; M2 += (s[i][1] + s[i][3]) + n * (d0 * d0 + d1 * d1); }
    const float rstd = __builtin_amdgcn_rsqf(M2 * (1.f / DM) + LN_EPS);
    u32x4 xo;
#pragma unroll
    for (int j = 0; j < 4; ++j) xo[j] = pk_bf16((x[2 * j] - mean) * rstd, (x[2 * j + 1] - mean) * rstd);
    if (on) lds_st<u32x4>(L, L_T + r8 * T_PITCH + col * 2, xo);
  }
  WAIT_L0(); BAR();
  if (layer == 0) xn_store(L, rW, OFF_XN + ACT_B, b, tid);
  WAIT_V0(); BAR();
}

__global__ __launch_bounds__(NTHR, 3) void fnet_swiglu_persistent(Params p) {
  extern __shared__ __attribute__((aligned(16))) char smem[];
  LAS char* L = (LAS char*)smem;
  const int w = __builtin_amdgcn_readfirstlane(threadIdx.x >> 6), lane = threadIdx.x & 63;
  const Roles R(blockIdx.x);
  if (w < 8) {
    const rsrc_t rW = __builtin_amdgcn_make_buffer_rsrc((void*)p.ws, 0, WS_USED, 0x00020000);
    p0_comp(p, R, L, rW, w, lane TR_PASS);
#pragma unroll 1
    for (int layer = 0; layer < 2; ++layer) {
      s_comp(p, R, L, rW, layer, w, fresh(lane) TR_PASS);
      h_comp(p, R, L, rW, layer, w, fresh(lane) TR_PASS);
      gd_comp(p, R, L, rW, layer, w, fresh(lane) TR_PASS);
      r_comp(p, R, L, rW, layer, w, fresh(lane) TR_PASS);
    }
  } else if (w == 9 || w == 10) {
    helper_role<0>(p, R, w - 9, lane TR_PASS);
  } else if (w == 8) {
    sync_role(p, R, true, lane TR_PASS);
  } else {
    helper_role<1>(p, R, 0, lane TR_PASS);
  }
}

}

extern "C" void kernel_launch(void* const* d_in, const int* in_sizes, int n_in, void* d_out, int out_size, void* d_ws, size_t ws_size,
                              hipStream_t stream) {
  (void)in_sizes; (void)n_in; (void)out_size; (void)ws_size;
  fk::Params p;
  p.x0 = d_in[0]; p.Cr = d_in[1]; p.Ci = d_in[2]; p.Wr = d_in[3]; p.Wi = d_in[4];
  p.Wg = d_in[6]; p.Wu = d_in[7]; p.Wd = d_in[8];
  p.out = d_out; p.ws = (char*)d_ws;
  (void)hipFuncSetAttribute((const void*)fk::fnet_swiglu_persistent, hipFuncAttributeMaxDynamicSharedMemorySize, fk::LDS_BYTES);
  fk::fnet_swiglu_persistent<<<fk::NWG, fk::NTHR, fk::LDS_BYTES, stream>>>(p);
}
```
